```python
import torch

def _softmax(x, axis=-1):
    return torch.softmax(x, dim=axis)
def _gelu(x):
    return torch.nn.functional.gelu(x, approximate="tanh")
def _silu(x):
    return torch.nn.functional.silu(x)
def _sigmoid(x):
    return torch.sigmoid(x)
def _layernorm(x, axis=-1, eps=1e-5):
    m = x.mean(dim=axis, keepdim=True)
    v = x.var(dim=axis, keepdim=True, unbiased=False)
    return (x - m) / torch.sqrt(v + eps)
def _scatter_add(tgt, idx, src, axis=0):
    return tgt.clone().index_add_(axis, idx, src)
def _segment_sum(src, seg, n):
    out = torch.zeros((n,) + src.shape[1:], dtype=src.dtype)
    return out.index_add_(0, seg, src)
def _pad(x, pw):
    flat = tuple(v for pair in reversed(pw) for v in pair)
    return torch.nn.functional.pad(x, flat)

kernel_name = 'standalone__paged_block_table_kv_radixattention__decode'

def setup_inputs():

    _w = (lambda a: (lambda t: t.to(torch.bfloat16) if t.is_floating_point() else t)(torch.as_tensor(a)))
    B, H, Hkv, Dh = 8, 32, 8, 128
    Lk = 8192
    bs = 16
    nb = max(2, Lk // bs)
    P = max(1, nb // 2)
    n_phys = P + B * (nb - P)
    shared = torch.arange(P, dtype=torch.int64)
    priv = P + torch.arange(B * (nb - P), dtype=torch.int64).reshape(B, nb - P)
    bt = torch.cat([torch.tile(shared[None, :], (B, 1)), priv], dim=1)
    ctx = nb * bs - (torch.arange(1, B + 1, dtype=torch.int64) * 2654435761) % max(1, (nb * bs - P * bs) // 2)
    return {
        "q": _w((lambda *s: torch.randn(*s, dtype=torch.bfloat16) * 0.8)(B, H, Dh)),
        "k_cache": _w((lambda *s: torch.randn(*s, dtype=torch.bfloat16) * 0.8)(n_phys, bs, Hkv, Dh)),
        "v_cache": _w((lambda *s: torch.randn(*s, dtype=torch.bfloat16) * 0.05)(n_phys, bs, Hkv, Dh)),
        "block_table": _w(bt.to(torch.int64)),
        "ctx_lens": _w(torch.as_tensor(ctx).to(torch.int64)),
    }

def reference(q, k_cache, v_cache, block_table, ctx_lens):
    B, H, Dh = q.shape
    n_phys, bs, Hkv, _ = k_cache.shape
    nb = block_table.shape[1]
    Lk = nb * bs
    rep = H // Hkv
    k = k_cache[block_table].reshape(B, Lk, Hkv, Dh)
    v = v_cache[block_table].reshape(B, Lk, Hkv, Dh)
    k = (lambda x, n, axis: torch.repeat_interleave(x, n, dim=axis))(k, rep, axis=2)
    v = (lambda x, n, axis: torch.repeat_interleave(x, n, dim=axis))(v, rep, axis=2)
    scores = torch.einsum("bhd,bkhd->bhk", q, k) * (Dh ** -0.5)
    pos = torch.arange(Lk)
    valid = pos[None, :] < ctx_lens[:, None]
    scores = torch.where(valid[:, None, :], scores, -1e9)
    p = _softmax(scores, axis=-1)
    return torch.einsum("bhk,bkhd->bhd", p, v)
```

```cpp
#include <hip/hip_runtime.h>
#include <hip/hip_bf16.h>
#include <stdint.h>
#include <float.h>

#define KV_NT 1
#define HEAD_LOW 1
#define W_OWN 0
#define W_SHR 0

namespace {
constexpr int B = 8, H = 32, HKV = 8, G = 4, D = 128, PAGE = 16, NB = 512, LK = NB * PAGE;
constexpr int WPH = 32;
constexpr int NWAVE = 8, NTHR = NWAVE * 64;
constexpr int PAGE_BYTES = PAGE * HKV * D * 2;
constexpr float CSC = 0.088388347648318440f * 1.4426950408889634f;
constexpr int PREC = 132 * 4;
constexpr int L_RING = 0;
constexpr int L_TBL = NWAVE * 16384;
constexpr int L_EXT = L_TBL + B * NB * 4;
constexpr int L_ML = L_EXT + 4 * 4 * 512;
constexpr int L_FM = L_ML + 12 * 32 * 8;
constexpr int L_END = L_FM + 64;
constexpr int WS_EPOCH = 0;
constexpr int WS_FLAG = 4096;
constexpr int WS_PART = WS_FLAG + HKV * B * WPH * 8;
constexpr int WS_BYTES = WS_PART + HKV * WPH * 32 * PREC;
constexpr unsigned long long TAG_STEP = 0x9E3779B97F4A7C15ull;

typedef __attribute__((ext_vector_type(4))) unsigned u32x4;
typedef __attribute__((ext_vector_type(2))) unsigned u32x2;
typedef __attribute__((ext_vector_type(4))) float f32x4;
typedef __attribute__((ext_vector_type(2))) float f32x2;
typedef __attribute__((ext_vector_type(8))) __bf16 bf16x8;
typedef __attribute__((ext_vector_type(4))) short s16x4;
typedef __attribute__((address_space(3))) unsigned char* ldsp;
typedef __attribute__((address_space(1))) unsigned long long* gu64p;

__device__ __forceinline__ unsigned cvtpk(float lo, float hi) {
  typedef __attribute__((ext_vector_type(2))) __bf16 bf16x2;
  bf16x2 r = {(__bf16)lo, (__bf16)hi};
  return __builtin_bit_cast(unsigned, r);
}
__device__ __forceinline__ float xg_max(float x) {
  auto s = __builtin_amdgcn_permlane16_swap(__float_as_uint(x), __float_as_uint(x), false, false);
  x = fmaxf(__uint_as_float(s[0]), __uint_as_float(s[1]));
  auto t = __builtin_amdgcn_permlane32_swap(__float_as_uint(x), __float_as_uint(x), false, false);
  return fmaxf(__uint_as_float(t[0]), __uint_as_float(t[1]));
}
__device__ __forceinline__ float xg_sum(float x) {
  auto s = __builtin_amdgcn_permlane16_swap(__float_as_uint(x), __float_as_uint(x), false, false);
  x = __uint_as_float(s[0]) + __uint_as_float(s[1]);
  auto t = __builtin_amdgcn_permlane32_swap(__float_as_uint(x), __float_as_uint(x), false, false);
  return __uint_as_float(t[0]) + __uint_as_float(t[1]);
}
template <typename T> __device__ __forceinline__ T lds_ld(ldsp p, int off) { return *(__attribute__((address_space(3))) T*)(p + off); }
template <typename T> __device__ __forceinline__ void lds_st(ldsp p, int off, T v) { *(__attribute__((address_space(3))) T*)(p + off) = v; }

#define KVPOL " nt"
__device__ __forceinline__ void dma4(const int (&vo)[4], u32x4 rs, unsigned soff, unsigned ldsaddr) {
  asm volatile("s_mov_b32 m0, %6\n\ts_nop 4\n\t"
               "buffer_load_dwordx4 %0, %4, %5 offen" KVPOL " lds\n\t"
               "buffer_load_dwordx4 %1, %4, %5 offen offset:1024" KVPOL " lds\n\t"
               "buffer_load_dwordx4 %2, %4, %5 offen offset:2048" KVPOL " lds\n\t"
               "buffer_load_dwordx4 %3, %4, %5 offen offset:3072" KVPOL " lds"
               :: "v"(vo[0]), "v"(vo[1]), "v"(vo[2]), "v"(vo[3]), "s"(rs), "s"(soff), "s"(ldsaddr) : "memory");
}

__global__ void __launch_bounds__(NTHR, 2)
attn_decode(const unsigned char* __restrict__ q, const unsigned char* __restrict__ kc, const unsigned char* __restrict__ vc,
            const int* __restrict__ bt, const int* __restrict__ ctxl, unsigned char* ws, unsigned short* __restrict__ out, long long nphys) {
  __shared__ __attribute__((aligned(16))) unsigned char smem[L_END];
  const ldsp lds = (ldsp)smem;
  const int tid = threadIdx.x, lane = tid & 63;
  const int w = __builtin_amdgcn_readfirstlane(tid >> 6);
  const int g = lane >> 4, c = lane & 15;
  const int kvh = blockIdx.x & 7, wi = blockIdx.x >> 3;
  int e[B];
#pragma unroll
  for (int b = 0; b < B; ++b) e[b] = bt[b * NB + tid];
  unsigned long long tag = __hip_atomic_load((gu64p)(ws + WS_EPOCH) + kvh * B + (lane & 7), __ATOMIC_RELAXED, __HIP_MEMORY_SCOPE_AGENT) + TAG_STEP;
  u32x4 qf[2][4];
#pragma unroll
  for (int x = 0; x < 2; ++x)
#pragma unroll
    for (int kk = 0; kk < 4; ++kk)
      qf[x][kk] = *reinterpret_cast<const u32x4*>(q + (((x * 4 + (c >> 2)) * H + kvh * G + (c & 3)) * D + kk * 32 + g * 8) * 2);
  int limv[2] = {ctxl[c >> 2], ctxl[4 + (c >> 2)]};
  int ctx[B], np[B], npmax = 0;
#pragma unroll
  for (int b = 0; b < B; ++b) { int t = ctxl[b]; t = t < 0 ? 0 : (t > LK ? LK : t); ctx[b] = t; np[b] = (t + PAGE - 1) / PAGE; npmax = max(npmax, np[b]); }
  bool mis = false;
#pragma unroll
  for (int b = 0; b < B; ++b) { lds_st<int>(lds, L_TBL + (b * NB + tid) * 4, e[b]); mis |= (e[b] != e[0]); }
  {
    const unsigned long long bal = __ballot(mis);
    const int first = bal ? (w * 64 + __builtin_ctzll(bal)) : NB;
    if (lane == 0) lds_st<int>(lds, L_FM + w * 4, first);
  }
  __syncthreads();
  int S;
  {
    const u32x4 f0 = lds_ld<u32x4>(lds, L_FM), f1 = lds_ld<u32x4>(lds, L_FM + 16);
    const unsigned s = min(min(min(f0[0], f0[1]), min(f0[2], f0[3])), min(min(f1[0], f1[1]), min(f1[2], f1[3])));
    S = min(__builtin_amdgcn_readfirstlane((int)s), npmax);
  }
  int off[B + 1];
  off[0] = S;
#pragma unroll
  for (int b = 0; b < B; ++b) off[b + 1] = off[b] + max(0, np[b] - S);
  const int Wp = off[B];
  int cj, ownmask;
  {
    int mid = off[0] + off[1];
#pragma unroll
    for (int b = 1; b < B; ++b) mid = (lane & 7) == b ? off[b] + off[b + 1] : mid;
    const float rw = 16.f * __builtin_amdgcn_rcpf((float)max(Wp, 1));
    const int own = min((int)((float)mid * rw), WPH - 1);
    ownmask = (int)(__ballot(own == wi) & 0xFF);
    unsigned cm = 0;
#pragma unroll
    for (int b = 0; b < B; ++b) cm |= 1u << __builtin_amdgcn_readlane(own, b);
    const int nsh = (int)((float)(2 * S) * rw);
    const int j = min(lane, WPH);
    const int cum = 64 * j + W_OWN * __builtin_popcount(cm & (unsigned)((1ull << j) - 1)) - W_SHR * min(j, nsh);
    const int tot = __builtin_amdgcn_readlane(cum, WPH);
    cj = min((int)((float)(Wp * cum) * __builtin_amdgcn_rcpf((float)tot)), Wp);
    cj = lane >= WPH ? Wp : cj;
  }
  const int c0 = __builtin_amdgcn_readlane(cj, wi), c1 = __builtin_amdgcn_readlane(cj, wi + 1);
  const int n = (c1 - c0 - w + NWAVE - 1) >> 3;
  unsigned dphys; int djb;
  {
    const int f = max(min(c0 + w + NWAVE * lane, Wp - 1), 0);
    int idx = f;
#pragma unroll
    for (int b = 0; b < B; ++b) idx = f >= off[b] ? f + (S + b * NB - off[b]) : idx;
    int ph = lds_ld<int>(lds, L_TBL + idx * 4);
    ph = ph < 0 ? ph + (int)nphys : ph;
    dphys = (unsigned)ph * PAGE_BYTES;
    djb = ((idx & (NB - 1)) * PAGE) | (f < S ? 0xFF0000 : 0x10000 << (idx >> 9));
  }
  int kvo[4], vvo[4];
#pragma unroll
  for (int t = 0; t < 4; ++t) {
    const int row = 4 * t + g;
    kvo[t] = row * 2048 + ((c ^ row) * 16) + kvh * 256 - t * 1024;
    vvo[t] = row * 2048 + ((c ^ ((row & 7) << 1)) * 16) + kvh * 256 - t * 1024;
  }
  const unsigned cbytes = (unsigned)nphys * PAGE_BYTES;
  u32x4 rk = {(unsigned)(uintptr_t)kc, (unsigned)((uintptr_t)kc >> 32), 0, 0x00020000u};
  u32x4 rv = {(unsigned)(uintptr_t)vc, (unsigned)((uintptr_t)vc >> 32), 0, 0x00020000u};
  const unsigned ring = (unsigned)(uintptr_t)lds + L_RING + w * 16384;
  asm volatile("" : "+v"(qf[0][0]), "+v"(qf[0][1]), "+v"(qf[0][2]), "+v"(qf[0][3]), "+v"(qf[1][0]), "+v"(qf[1][1]), "+v"(qf[1][2]), "+v"(qf[1][3]),
               "+v"(tag), "+v"(limv[0]), "+v"(limv[1]));
#pragma unroll
  for (int u = 0; u < 2; ++u) {
    const unsigned so = __builtin_amdgcn_readlane(dphys, u);
    rk[2] = rv[2] = u < n ? cbytes : 0;
    dma4(kvo, rk, so, ring + u * 8192);
    dma4(vvo, rv, so, ring + u * 8192 + 4096);
  }
  int wgmask = (c0 < S && c0 < c1) ? 0xFF : 0;
#pragma unroll
  for (int b = 0; b < B; ++b) if (max(c0, off[b]) < min(c1, off[b + 1])) wgmask |= 1 << b;
  int bbit[2];
#pragma unroll
  for (int x = 0; x < 2; ++x) {
    limv[x] = min(max(limv[x], 0), LK) - 4 * g;
    bbit[x] = 1 << (16 + 4 * x + (c >> 2));
  }
  int ka = L_RING + w * 16384 + c * 256 + ((g ^ c) * 16);
  int va = L_RING + w * 16384 + 4096 + (4 * g + (c >> 2)) * 256 + ((((4 * g + (c >> 2)) & 7) << 5) | ((c & 3) << 3));

  f32x4 acc[2][8];
  float m[2], l[2];
#pragma unroll
  for (int x = 0; x < 2; ++x) { m[x] = -FLT_MAX; l[x] = 0.f;
#pragma unroll
    for (int d = 0; d < 8; ++d) acc[x][d] = f32x4{0.f, 0.f, 0.f, 0.f}; }

  unsigned slot = ring;
  for (int i = 0; i < n; ++i) {
    const int jb = __builtin_amdgcn_readlane(djb, i);
    const unsigned so = __builtin_amdgcn_readlane(dphys, i + 2);
    rk[2] = rv[2] = i + 2 < n ? cbytes : 0;
    asm volatile("s_waitcnt vmcnt(12)" ::: "memory");
    u32x4 kf[4];
#pragma unroll
    for (int kk = 0; kk < 4; ++kk) kf[kk] = lds_ld<u32x4>(lds, ka ^ (kk * 64));
    asm volatile("s_waitcnt lgkmcnt(0)" : "+v"(kf[0]), "+v"(kf[1]), "+v"(kf[2]), "+v"(kf[3]) :: "memory");
    dma4(kvo, rk, so, slot);
    u32x2 pb[2];
#pragma unroll
    for (int x = 0; x < 2; ++x) {
      if (jb & (0xF0000 << (4 * x))) {
        f32x4 s = {0.f, 0.f, 0.f, 0.f};
#pragma unroll
        for (int kk = 0; kk < 4; ++kk)
          s = __builtin_amdgcn_mfma_f32_16x16x32_bf16(__builtin_bit_cast(bf16x8, kf[kk]), __builtin_bit_cast(bf16x8, qf[x][kk]), s, 0, 0, 0);
        const int lim = (jb & bbit[x]) ? limv[x] - (jb & 0xFFFF) : 0;
#pragma unroll
        for (int r = 0; r < 4; ++r) s[r] = (r < lim) ? s[r] : -INFINITY;
        float mx = fmaxf(fmaxf(s[0], s[1]), fmaxf(s[2], s[3]));
        mx = xg_max(mx) * CSC;
        const float mn = fmaxf(m[x], mx);
        const float al = __builtin_amdgcn_exp2f(m[x] - mn);
        m[x] = mn;
        float p[4];
#pragma unroll
        for (int r = 0; r < 4; ++r) p[r] = __builtin_amdgcn_exp2f(__builtin_fmaf(s[r], CSC, -mn));
        l[x] = __builtin_fmaf(l[x], al, (p[0] + p[1]) + (p[2] + p[3]));
        pb[x] = u32x2{cvtpk(p[0], p[1]), cvtpk(p[2], p[3])};
#pragma unroll
        for (int d = 0; d < 8; ++d) acc[x][d] *= al;
      }
    }
    asm volatile("s_waitcnt vmcnt(12)" ::: "memory");
    s16x4 vt[8];
#pragma unroll
    for (int d = 0; d < 8; ++d) {
      typedef __attribute__((ext_vector_type(4))) short v4i16;
      vt[d] = __builtin_amdgcn_ds_read_tr16_b64_v4i16((__attribute__((address_space(3))) v4i16*)(lds + (va ^ (d * 32))));
    }
    asm volatile("s_waitcnt lgkmcnt(0)" : "+v"(vt[0]), "+v"(vt[1]), "+v"(vt[2]), "+v"(vt[3]), "+v"(vt[4]), "+v"(vt[5]), "+v"(vt[6]), "+v"(vt[7]) :: "memory");
    dma4(vvo, rv, so, slot + 4096);
#pragma unroll
    for (int x = 0; x < 2; ++x) {
      if (jb & (0xF0000 << (4 * x))) {
#pragma unroll
        for (int d = 0; d < 8; ++d)
          acc[x][d] = __builtin_amdgcn_mfma_f32_16x16x16bf16_1k(vt[d], __builtin_bit_cast(s16x4, pb[x]), acc[x][d], 0, 0, 0);
      }
    }
    slot ^= 8192; ka ^= 8192; va ^= 8192;
  }
  asm volatile("s_waitcnt vmcnt(0)" ::: "memory");
#pragma unroll
  for (int x = 0; x < 2; ++x) {
    if (wgmask & (0xF << (4 * x))) {
      const float lt = xg_sum(l[x]);
      if ((wgmask << 16) & bbit[x]) {
        const int rb = L_RING + w * 16384 + (x * 16 + c) * 512;
#pragma unroll
        for (int d = 0; d < 8; ++d) lds_st<f32x4>(lds, rb + (((4 * d + g) ^ (c & 7)) * 16), acc[x][d]);
        if (g == 0) lds_st<f32x2>(lds, L_ML + (w * 32 + x * 16 + c) * 8, f32x2{m[x], lt});
      }
    }
  }
  const auto rw = __builtin_amdgcn_make_buffer_rsrc(ws, 0, WS_BYTES, 0x00020000);
  const int r4 = (tid >> 5) & 3, ch = tid & 31;
  int pub = wgmask & ~ownmask;
  if (pub) {
    __syncthreads();
    int pm = pub;
    for (int k = w >> 1; k > 0; --k) pm &= pm - 1;
    while (pm) {
      const int row = __builtin_ctz(pm) * 4 + r4;
      pm &= pm - 1; pm &= pm - 1; pm &= pm - 1; pm &= pm - 1;
      f32x2 ml[NWAVE]; f32x4 ow[NWAVE];
      float M = -FLT_MAX;
#pragma unroll
      for (int ww = 0; ww < NWAVE; ++ww) {
        ml[ww] = lds_ld<f32x2>(lds, L_ML + (ww * 32 + row) * 8);
        ow[ww] = lds_ld<f32x4>(lds, L_RING + ww * 16384 + row * 512 + ((ch ^ (row & 7)) * 16));
        M = fmaxf(M, ml[ww][0]);
      }
      float L = 0.f; f32x4 o = {0.f, 0.f, 0.f, 0.f};
#pragma unroll
      for (int ww = 0; ww < NWAVE; ++ww) {
        const float wt = __builtin_amdgcn_exp2f(ml[ww][0] - M);
        L = __builtin_fmaf(wt, ml[ww][1], L);
        o += wt * ow[ww];
      }
      const int dst = WS_PART + ((kvh * WPH + wi) * 32 + row) * PREC;
      __builtin_amdgcn_raw_buffer_store_b128(__builtin_bit_cast(u32x4, o), rw, dst + ch * 16, 0, 16);
      if (ch == 0) __builtin_amdgcn_raw_buffer_store_b64(u32x2{__float_as_uint(M), __float_as_uint(L)}, rw, dst + 512, 0, 16);
    }
    asm volatile("s_waitcnt vmcnt(0)" ::: "memory");
    __syncthreads();
    if (tid < B && ((pub >> tid) & 1))
      __hip_atomic_store((gu64p)(ws + WS_FLAG) + (kvh * B + tid) * WPH + wi, tag, __ATOMIC_RELAXED, __HIP_MEMORY_SCOPE_AGENT);
  }
  for (int om = ownmask; om; om &= om - 1) {
    const int b = __builtin_ctz(om);
    const unsigned long long tb = ((unsigned long long)(unsigned)__builtin_amdgcn_readlane((unsigned)(tag >> 32), b) << 32) | (unsigned)__builtin_amdgcn_readlane((unsigned)tag, b);
    if (w < 4) {
      int ob = off[0], oe = off[1];
#pragma unroll
      for (int bb = 1; bb < B; ++bb) if (b == bb) { ob = off[bb]; oe = off[bb + 1]; }
      const int cn = __builtin_amdgcn_ds_bpermute(min(lane + 1, 63) * 4, cj);
      const bool con = (cj < S && cj < cn) || max(cj, ob) < min(cn, oe);
      unsigned pend = (unsigned)__ballot(con && lane < WPH && lane != wi && (lane & 3) == w);
      float M = -FLT_MAX, L = 0.f; f32x4 o0 = {0.f, 0.f, 0.f, 0.f}, o1 = o0;
      for (int spin = 0; pend && spin < (1 << 18); ++spin) {
        const unsigned long long fl = __hip_atomic_load((gu64p)(ws + WS_FLAG) + (kvh * B + b) * WPH + (lane & 31), __ATOMIC_RELAXED, __HIP_MEMORY_SCOPE_AGENT);
        unsigned rdy = (unsigned)__ballot(fl == tb) & pend;
        if (!rdy) { __builtin_amdgcn_s_sleep(8); continue; }
        asm volatile("" ::: "memory");
        u32x4 pa[3], pc[3]; u32x2 pq[3]; bool ok[3];
#pragma unroll
        for (int k = 0; k < 3; ++k) {
          ok[k] = rdy != 0;
          const int j = ok[k] ? __builtin_ctz(rdy) : 0;
          pend &= ~(rdy & -rdy); rdy &= rdy - 1;
          const int src = ok[k] ? WS_PART + ((kvh * WPH + j) * 32 + b * 4) * PREC : WS_BYTES;
          pa[k] = __builtin_amdgcn_raw_buffer_load_b128(rw, src + g * PREC + c * 32, 0, 16);
          pc[k] = __builtin_amdgcn_raw_buffer_load_b128(rw, src + g * PREC + c * 32 + 16, 0, 16);
          pq[k] = __builtin_amdgcn_raw_buffer_load_b64(rw, src + g * PREC + 512, 0, 16);
        }
#pragma unroll
        for (int k = 0; k < 3; ++k) {
          const float mk = ok[k] ? __uint_as_float(pq[k][0]) : -FLT_MAX;
          const float Mn = fmaxf(M, mk);
          const float a = __builtin_amdgcn_exp2f(M - Mn), wt = __builtin_amdgcn_exp2f(mk - Mn);
          L = L * a + wt * __uint_as_float(pq[k][1]);
          o0 = o0 * a + wt * __builtin_bit_cast(f32x4, pa[k]);
          o1 = o1 * a + wt * __builtin_bit_cast(f32x4, pc[k]);
          M = Mn;
        }
      }
      lds_st<f32x4>(lds, L_EXT + (w * 4 + g) * 512 + c * 32, o0);
      lds_st<f32x4>(lds, L_EXT + (w * 4 + g) * 512 + c * 32 + 16, o1);
      if (c == 0) lds_st<f32x2>(lds, L_ML + ((NWAVE + w) * 32 + g) * 8, f32x2{M, L});
    }
    __syncthreads();
    if (tid < 128) {
      const int row = b * 4 + r4;
      const bool mine = (wgmask >> b) & 1;
      f32x2 ml[12]; f32x4 ow[12];
      float M = -FLT_MAX;
#pragma unroll
      for (int ww = 0; ww < 12; ++ww) {
        if (ww < NWAVE) {
          ml[ww] = mine ? lds_ld<f32x2>(lds, L_ML + (ww * 32 + row) * 8) : f32x2{-FLT_MAX, 0.f};
          ow[ww] = mine ? lds_ld<f32x4>(lds, L_RING + ww * 16384 + row * 512 + ((ch ^ (row & 7)) * 16)) : f32x4{0.f, 0.f, 0.f, 0.f};
        } else {
          ml[ww] = lds_ld<f32x2>(lds, L_ML + (ww * 32 + r4) * 8);
          ow[ww] = lds_ld<f32x4>(lds, L_EXT + ((ww - NWAVE) * 4 + r4) * 512 + ch * 16);
        }
        M = fmaxf(M, ml[ww][0]);
      }
      float L = 0.f; f32x4 o = {0.f, 0.f, 0.f, 0.f};
#pragma unroll
      for (int ww = 0; ww < 12; ++ww) {
        const float wt = __builtin_amdgcn_exp2f(ml[ww][0] - M);
        L = __builtin_fmaf(wt, ml[ww][1], L);
        o += wt * ow[ww];
      }
      int cx = ctx[0];
#pragma unroll
      for (int bb = 1; bb < B; ++bb) if (b == bb) cx = ctx[bb];
      constexpr float MASKED = -998244352.0f * 1.4426950408889634f;
      if (cx < LK && MASKED - M > -200.f) {
        f32x4 sv = {0.f, 0.f, 0.f, 0.f};
        for (int t = cx; t < LK; ++t) {
          int ph = bt[b * NB + (t >> 4)];
          ph = ph < 0 ? ph + (int)nphys : ph;
          const u32x2 vv = *reinterpret_cast<const u32x2*>(vc + (size_t)ph * PAGE_BYTES + (t & 15) * 2048 + kvh * 256 + ch * 8);
          sv += f32x4{__uint_as_float(vv[0] << 16), __uint_as_float(vv[0] & 0xFFFF0000u), __uint_as_float(vv[1] << 16), __uint_as_float(vv[1] & 0xFFFF0000u)};
        }
        const float Mn = fmaxf(M, MASKED);
        const float a = __builtin_amdgcn_exp2f(M - Mn), wt = __builtin_amdgcn_exp2f(MASKED - Mn);
        L = L * a + wt * (float)(LK - cx);
        o = o * a + wt * sv;
      }
      const float inv = 1.f / L;
      *reinterpret_cast<u32x2*>(out + (b * H + kvh * G + r4) * D + ch * 4) = u32x2{cvtpk(o[0] * inv, o[1] * inv), cvtpk(o[2] * inv, o[3] * inv)};
      if (tid == 0) __hip_atomic_store((gu64p)(ws + WS_EPOCH) + kvh * B + b, tb, __ATOMIC_RELAXED, __HIP_MEMORY_SCOPE_AGENT);
    }
    if (om & (om - 1)) __syncthreads();
  }
}
}

extern "C" void kernel_launch(void* const* d_in, const int* in_sizes, int n_in, void* d_out, int out_size, void* d_ws, size_t ws_size,
                              hipStream_t stream) {
  attn_decode<<<HKV * WPH, NTHR, 0, stream>>>((const unsigned char*)d_in[0], (const unsigned char*)d_in[1], (const unsigned char*)d_in[2],
                                              (const int*)d_in[3], (const int*)d_in[4], (unsigned char*)d_ws, (unsigned short*)d_out,
                                              (long long)(in_sizes[1] / (PAGE * HKV * D)));
}
```

```cpp
#define PROBE 0
#define PROBE_COND (blockIdx.x == 0 && w == 0)
#include <hip/hip_runtime.h>
#include <hip/hip_bf16.h>
#include <stdint.h>
#include <float.h>

#define KV_NT 1
#define HEAD_LOW 1
#define W_OWN 6
#define W_SHR 3

namespace {
constexpr int B = 8, H = 32, HKV = 8, G = 4, D = 128, PAGE = 16, NB = 512, LK = NB * PAGE;
constexpr int WPH = 32;
constexpr int NWAVE = 8, NTHR = NWAVE * 64;
constexpr int PAGE_BYTES = PAGE * HKV * D * 2;
constexpr float CSC = 0.088388347648318440f * 1.4426950408889634f;
constexpr int PREC = 132 * 4;
constexpr int L_RING = 0;
constexpr int L_TBL = NWAVE * 16384;
constexpr int L_EXT = L_TBL + B * NB * 4;
constexpr int L_ML = L_EXT + 4 * 4 * 512;
constexpr int L_FM = L_ML + 12 * 32 * 8;
constexpr int L_END = L_FM + 64;
constexpr int WS_EPOCH = 0;
constexpr int WS_FLAG = 4096;
constexpr int WS_PART = WS_FLAG + HKV * B * WPH * 8;
constexpr int WS_BYTES = WS_PART + HKV * WPH * 32 * PREC;
constexpr unsigned long long TAG_STEP = 0x9E3779B97F4A7C15ull;

typedef __attribute__((ext_vector_type(4))) unsigned u32x4;
typedef __attribute__((ext_vector_type(2))) unsigned u32x2;
typedef __attribute__((ext_vector_type(4))) float f32x4;
typedef __attribute__((ext_vector_type(2))) float f32x2;
typedef __attribute__((ext_vector_type(8))) __bf16 bf16x8;
typedef __attribute__((ext_vector_type(4))) short s16x4;
typedef __attribute__((address_space(3))) unsigned char* ldsp;
typedef __attribute__((address_space(1))) unsigned long long* gu64p;

__device__ __forceinline__ unsigned cvtpk(float lo, float hi) {
  typedef __attribute__((ext_vector_type(2))) __bf16 bf16x2;
  bf16x2 r = {(__bf16)lo, (__bf16)hi};
  return __builtin_bit_cast(unsigned, r);
}
__device__ __forceinline__ float xg_max(float x) {
  auto s = __builtin_amdgcn_permlane16_swap(__float_as_uint(x), __float_as_uint(x), false, false);
  x = fmaxf(__uint_as_float(s[0]), __uint_as_float(s[1]));
  auto t = __builtin_amdgcn_permlane32_swap(__float_as_uint(x), __float_as_uint(x), false, false);
  return fmaxf(__uint_as_float(t[0]), __uint_as_float(t[1]));
}
__device__ __forceinline__ float xg_sum(float x) {
  auto s = __builtin_amdgcn_permlane16_swap(__float_as_uint(x), __float_as_uint(x), false, false);
  x = __uint_as_float(s[0]) + __uint_as_float(s[1]);
  auto t = __builtin_amdgcn_permlane32_swap(__float_as_uint(x), __float_as_uint(x), false, false);
  return __uint_as_float(t[0]) + __uint_as_float(t[1]);
}
template <typename T> __device__ __forceinline__ T lds_ld(ldsp p, int off) { return *(__attribute__((address_space(3))) T*)(p + off); }
template <typename T> __device__ __forceinline__ void lds_st(ldsp p, int off, T v) { *(__attribute__((address_space(3))) T*)(p + off) = v; }

#define STAMP(i) do { if (i == PROBE) st_ = __builtin_amdgcn_s_memrealtime(); } while (0)
#define STAMP_OUT() do { if (PROBE_COND) while (__builtin_amdgcn_s_memrealtime() < st_ + 4000) __builtin_amdgcn_s_sleep(2); } while (0)
#define KVPOL " nt"
__device__ __forceinline__ void dma4(const int (&vo)[4], u32x4 rs, unsigned soff, unsigned ldsaddr) {
  asm volatile("s_mov_b32 m0, %6\n\ts_nop 4\n\t"
               "buffer_load_dwordx4 %0, %4, %5 offen" KVPOL " lds\n\t"
               "buffer_load_dwordx4 %1, %4, %5 offen offset:1024" KVPOL " lds\n\t"
               "buffer_load_dwordx4 %2, %4, %5 offen offset:2048" KVPOL " lds\n\t"
               "buffer_load_dwordx4 %3, %4, %5 offen offset:3072" KVPOL " lds"
               :: "v"(vo[0]), "v"(vo[1]), "v"(vo[2]), "v"(vo[3]), "s"(rs), "s"(soff), "s"(ldsaddr) : "memory");
}

__global__ void __launch_bounds__(NTHR, 2)
attn_decode(const unsigned char* __restrict__ q, const unsigned char* __restrict__ kc, const unsigned char* __restrict__ vc,
            const int* __restrict__ bt, const int* __restrict__ ctxl, unsigned char* ws, unsigned short* __restrict__ out, long long nphys) {
  __shared__ __attribute__((aligned(16))) unsigned char smem[L_END];
  const ldsp lds = (ldsp)smem;
  unsigned long long st_ = 0;
  STAMP(0);
  const int tid = threadIdx.x, lane = tid & 63;
  const int w = __builtin_amdgcn_readfirstlane(tid >> 6);
  const int g = lane >> 4, c = lane & 15;
  const int kvh = blockIdx.x & 7, wi = blockIdx.x >> 3;
  int e[B];
#pragma unroll
  for (int b = 0; b < B; ++b) e[b] = bt[b * NB + tid];
  unsigned long long tag = __hip_atomic_load((gu64p)(ws + WS_EPOCH) + kvh * B + (lane & 7), __ATOMIC_RELAXED, __HIP_MEMORY_SCOPE_AGENT) + TAG_STEP;
  u32x4 qf[2][4];
#pragma unroll
  for (int x = 0; x < 2; ++x)
#pragma unroll
    for (int kk = 0; kk < 4; ++kk)
      qf[x][kk] = *reinterpret_cast<const u32x4*>(q + (((x * 4 + (c >> 2)) * H + kvh * G + (c & 3)) * D + kk * 32 + g * 8) * 2);
  int limv[2] = {ctxl[c >> 2], ctxl[4 + (c >> 2)]};
  int ctx[B], np[B], npmax = 0;
#pragma unroll
  for (int b = 0; b < B; ++b) { int t = ctxl[b]; t = t < 0 ? 0 : (t > LK ? LK : t); ctx[b] = t; np[b] = (t + PAGE - 1) / PAGE; npmax = max(npmax, np[b]); }
  bool mis = false;
#pragma unroll
  for (int b = 0; b < B; ++b) { lds_st<int>(lds, L_TBL + (b * NB + tid) * 4, e[b]); mis |= (e[b] != e[0]); }
  {
    const unsigned long long bal = __ballot(mis);
    const int first = bal ? (w * 64 + __builtin_ctzll(bal)) : NB;
    if (lane == 0) lds_st<int>(lds, L_FM + w * 4, first);
  }
  __syncthreads();
  STAMP(1);
  int S;
  {
    const u32x4 f0 = lds_ld<u32x4>(lds, L_FM), f1 = lds_ld<u32x4>(lds, L_FM + 16);
    const unsigned s = min(min(min(f0[0], f0[1]), min(f0[2], f0[3])), min(min(f1[0], f1[1]), min(f1[2], f1[3])));
    S = min(__builtin_amdgcn_readfirstlane((int)s), npmax);
  }
  int off[B + 1];
  off[0] = S;
#pragma unroll
  for (int b = 0; b < B; ++b) off[b + 1] = off[b] + max(0, np[b] - S);
  const int Wp = off[B];
  int cj, ownmask;
  {
    int mid = off[0] + off[1];
#pragma unroll
    for (int b = 1; b < B; ++b) mid = (lane & 7) == b ? off[b] + off[b + 1] : mid;
    const float rw = 16.f * __builtin_amdgcn_rcpf((float)max(Wp, 1));
    const int own = min((int)((float)mid * rw), WPH - 1);
    ownmask = (int)(__ballot(own == wi) & 0xFF);
    unsigned cm = 0;
#pragma unroll
    for (int b = 0; b < B; ++b) cm |= 1u << __builtin_amdgcn_readlane(own, b);
    const int nsh = (int)((float)(2 * S) * rw);
    const int j = min(lane, WPH);
    const int cum = 64 * j + W_OWN * __builtin_popcount(cm & (unsigned)((1ull << j) - 1)) - W_SHR * min(j, nsh);
    const int tot = __builtin_amdgcn_readlane(cum, WPH);
    cj = min((int)((float)(Wp * cum) * __builtin_amdgcn_rcpf((float)tot)), Wp);
    cj = lane >= WPH ? Wp : cj;
  }
  const int c0 = __builtin_amdgcn_readlane(cj, wi), c1 = __builtin_amdgcn_readlane(cj, wi + 1);
  const int n = (c1 - c0 - w + NWAVE - 1) >> 3;
  unsigned dphys; int djb;
  {
    const int f = max(min(c0 + w + NWAVE * lane, Wp - 1), 0);
    int idx = f;
#pragma unroll
    for (int b = 0; b < B; ++b) idx = f >= off[b] ? f + (S + b * NB - off[b]) : idx;
    int ph = lds_ld<int>(lds, L_TBL + idx * 4);
    ph = ph < 0 ? ph + (int)nphys : ph;
    dphys = (unsigned)ph * PAGE_BYTES;
    djb = ((idx & (NB - 1)) * PAGE) | (f < S ? 0xFF0000 : 0x10000 << (idx >> 9));
  }
  int kvo[4], vvo[4];
#pragma unroll
  for (int t = 0; t < 4; ++t) {
    const int row = 4 * t + g;
    kvo[t] = row * 2048 + ((c ^ row) * 16) + kvh * 256 - t * 1024;
    vvo[t] = row * 2048 + ((c ^ ((row & 7) << 1)) * 16) + kvh * 256 - t * 1024;
  }
  const unsigned cbytes = (unsigned)nphys * PAGE_BYTES;
  u32x4 rk = {(unsigned)(uintptr_t)kc, (unsigned)((uintptr_t)kc >> 32), 0, 0x00020000u};
  u32x4 rv = {(unsigned)(uintptr_t)vc, (unsigned)((uintptr_t)vc >> 32), 0, 0x00020000u};
  const unsigned ring = (unsigned)(uintptr_t)lds + L_RING + w * 16384;
  asm volatile("" : "+v"(qf[0][0]), "+v"(qf[0][1]), "+v"(qf[0][2]), "+v"(qf[0][3]), "+v"(qf[1][0]), "+v"(qf[1][1]), "+v"(qf[1][2]), "+v"(qf[1][3]),
               "+v"(tag), "+v"(limv[0]), "+v"(limv[1]));
#pragma unroll
  for (int u = 0; u < 2; ++u) {
    const unsigned so = __builtin_amdgcn_readlane(dphys, u);
    rk[2] = rv[2] = u < n ? cbytes : 0;
    dma4(kvo, rk, so, ring + u * 8192);
    dma4(vvo, rv, so, ring + u * 8192 + 4096);
  }
  STAMP(2);
  int wgmask = (c0 < S && c0 < c1) ? 0xFF : 0;
#pragma unroll
  for (int b = 0; b < B; ++b) if (max(c0, off[b]) < min(c1, off[b + 1])) wgmask |= 1 << b;
  int bbit[2];
#pragma unroll
  for (int x = 0; x < 2; ++x) {
    limv[x] = min(max(limv[x], 0), LK) - 4 * g;
    bbit[x] = 1 << (16 + 4 * x + (c >> 2));
  }
  int ka = L_RING + w * 16384 + c * 256 + ((g ^ c) * 16);
  int va = L_RING + w * 16384 + 4096 + (4 * g + (c >> 2)) * 256 + ((((4 * g + (c >> 2)) & 7) << 5) | ((c & 3) << 3));

  f32x4 acc[2][8];
  float m[2], l[2];
#pragma unroll
  for (int x = 0; x < 2; ++x) { m[x] = -FLT_MAX; l[x] = 0.f;
#pragma unroll
    for (int d = 0; d < 8; ++d) acc[x][d] = f32x4{0.f, 0.f, 0.f, 0.f}; }

  unsigned slot = ring;
  for (int i = 0; i < n; ++i) {
    const int jb = __builtin_amdgcn_readlane(djb, i);
    const unsigned so = __builtin_amdgcn_readlane(dphys, i + 2);
    rk[2] = rv[2] = i + 2 < n ? cbytes : 0;
    asm volatile("s_waitcnt vmcnt(12)" ::: "memory");
    if (i == 0) STAMP(3);
    u32x4 kf[4];
#pragma unroll
    for (int kk = 0; kk < 4; ++kk) kf[kk] = lds_ld<u32x4>(lds, ka ^ (kk * 64));
    asm volatile("s_waitcnt lgkmcnt(0)" : "+v"(kf[0]), "+v"(kf[1]), "+v"(kf[2]), "+v"(kf[3]) :: "memory");
    dma4(kvo, rk, so, slot);
    u32x2 pb[2];
#pragma unroll
    for (int x = 0; x < 2; ++x) {
      if (jb & (0xF0000 << (4 * x))) {
        f32x4 s = {0.f, 0.f, 0.f, 0.f};
#pragma unroll
        for (int kk = 0; kk < 4; ++kk)
          s = __builtin_amdgcn_mfma_f32_16x16x32_bf16(__builtin_bit_cast(bf16x8, kf[kk]), __builtin_bit_cast(bf16x8, qf[x][kk]), s, 0, 0, 0);
        const int lim = (jb & bbit[x]) ? limv[x] - (jb & 0xFFFF) : 0;
#pragma unroll
        for (int r = 0; r < 4; ++r) s[r] = (r < lim) ? s[r] : -INFINITY;
        float mx = fmaxf(fmaxf(s[0], s[1]), fmaxf(s[2], s[3]));
        mx = xg_max(mx) * CSC;
        const float mn = fmaxf(m[x], mx);
        const float al = __builtin_amdgcn_exp2f(m[x] - mn);
        m[x] = mn;
        float p[4];
#pragma unroll
        for (int r = 0; r < 4; ++r) p[r] = __builtin_amdgcn_exp2f(__builtin_fmaf(s[r], CSC, -mn));
        l[x] = __builtin_fmaf(l[x], al, (p[0] + p[1]) + (p[2] + p[3]));
        pb[x] = u32x2{cvtpk(p[0], p[1]), cvtpk(p[2], p[3])};
#pragma unroll
        for (int d = 0; d < 8; ++d) acc[x][d] *= al;
      }
    }
    asm volatile("s_waitcnt vmcnt(12)" ::: "memory");
    s16x4 vt[8];
#pragma unroll
    for (int d = 0; d < 8; ++d) {
      typedef __attribute__((ext_vector_type(4))) short v4i16;
      vt[d] = __builtin_amdgcn_ds_read_tr16_b64_v4i16((__attribute__((address_space(3))) v4i16*)(lds + (va ^ (d * 32))));
    }
    asm volatile("s_waitcnt lgkmcnt(0)" : "+v"(vt[0]), "+v"(vt[1]), "+v"(vt[2]), "+v"(vt[3]), "+v"(vt[4]), "+v"(vt[5]), "+v"(vt[6]), "+v"(vt[7]) :: "memory");
    dma4(vvo, rv, so, slot + 4096);
#pragma unroll
    for (int x = 0; x < 2; ++x) {
      if (jb & (0xF0000 << (4 * x))) {
#pragma unroll
        for (int d = 0; d < 8; ++d)
          acc[x][d] = __builtin_amdgcn_mfma_f32_16x16x16bf16_1k(vt[d], __builtin_bit_cast(s16x4, pb[x]), acc[x][d], 0, 0, 0);
      }
    }
    slot ^= 8192; ka ^= 8192; va ^= 8192;
  }
  asm volatile("s_waitcnt vmcnt(0)" ::: "memory");
  STAMP(4);
#pragma unroll
  for (int x = 0; x < 2; ++x) {
    if (wgmask & (0xF << (4 * x))) {
      const float lt = xg_sum(l[x]);
      if ((wgmask << 16) & bbit[x]) {
        const int rb = L_RING + w * 16384 + (x * 16 + c) * 512;
#pragma unroll
        for (int d = 0; d < 8; ++d) lds_st<f32x4>(lds, rb + (((4 * d + g) ^ (c & 7)) * 16), acc[x][d]);
        if (g == 0) lds_st<f32x2>(lds, L_ML + (w * 32 + x * 16 + c) * 8, f32x2{m[x], lt});
      }
    }
  }
  const auto rw = __builtin_amdgcn_make_buffer_rsrc(ws, 0, WS_BYTES, 0x00020000);
  const int r4 = (tid >> 5) & 3, ch = tid & 31;
  int pub = wgmask & ~ownmask;
  if (pub) {
    __syncthreads();
    int pm = pub;
    for (int k = w >> 1; k > 0; --k) pm &= pm - 1;
    while (pm) {
      const int row = __builtin_ctz(pm) * 4 + r4;
      pm &= pm - 1; pm &= pm - 1; pm &= pm - 1; pm &= pm - 1;
      f32x2 ml[NWAVE]; f32x4 ow[NWAVE];
      float M = -FLT_MAX;
#pragma unroll
      for (int ww = 0; ww < NWAVE; ++ww) {
        ml[ww] = lds_ld<f32x2>(lds, L_ML + (ww * 32 + row) * 8);
        ow[ww] = lds_ld<f32x4>(lds, L_RING + ww * 16384 + row * 512 + ((ch ^ (row & 7)) * 16));
        M = fmaxf(M, ml[ww][0]);
      }
      float L = 0.f; f32x4 o = {0.f, 0.f, 0.f, 0.f};
#pragma unroll
      for (int ww = 0; ww < NWAVE; ++ww) {
        const float wt = __builtin_amdgcn_exp2f(ml[ww][0] - M);
        L = __builtin_fmaf(wt, ml[ww][1], L);
        o += wt * ow[ww];
      }
      const int dst = WS_PART + ((kvh * WPH + wi) * 32 + row) * PREC;
      __builtin_amdgcn_raw_buffer_store_b128(__builtin_bit_cast(u32x4, o), rw, dst + ch * 16, 0, 16);
      if (ch == 0) __builtin_amdgcn_raw_buffer_store_b64(u32x2{__float_as_uint(M), __float_as_uint(L)}, rw, dst + 512, 0, 16);
    }
    asm volatile("s_waitcnt vmcnt(0)" ::: "memory");
    __syncthreads();
    if (tid < B && ((pub >> tid) & 1))
      __hip_atomic_store((gu64p)(ws + WS_FLAG) + (kvh * B + tid) * WPH + wi, tag, __ATOMIC_RELAXED, __HIP_MEMORY_SCOPE_AGENT);
  }
  STAMP(5);
  for (int om = ownmask; om; om &= om - 1) {
    const int b = __builtin_ctz(om);
    const unsigned long long tb = ((unsigned long long)(unsigned)__builtin_amdgcn_readlane((unsigned)(tag >> 32), b) << 32) | (unsigned)__builtin_amdgcn_readlane((unsigned)tag, b);
    if (w < 4) {
      int ob = off[0], oe = off[1];
#pragma unroll
      for (int bb = 1; bb < B; ++bb) if (b == bb) { ob = off[bb]; oe = off[bb + 1]; }
      const int cn = __builtin_amdgcn_ds_bpermute(min(lane + 1, 63) * 4, cj);
      const bool con = (cj < S && cj < cn) || max(cj, ob) < min(cn, oe);
      unsigned pend = (unsigned)__ballot(con && lane < WPH && lane != wi && (lane & 3) == w);
      float M = -FLT_MAX, L = 0.f; f32x4 o0 = {0.f, 0.f, 0.f, 0.f}, o1 = o0;
      for (int spin = 0; pend && spin < (1 << 18); ++spin) {
        const unsigned long long fl = __hip_atomic_load((gu64p)(ws + WS_FLAG) + (kvh * B + b) * WPH + (lane & 31), __ATOMIC_RELAXED, __HIP_MEMORY_SCOPE_AGENT);
        unsigned rdy = (unsigned)__ballot(fl == tb) & pend;
        if (!rdy) { __builtin_amdgcn_s_sleep(8); continue; }
        asm volatile("" ::: "memory");
        u32x4 pa[3], pc[3]; u32x2 pq[3]; bool ok[3];
#pragma unroll
        for (int k = 0; k < 3; ++k) {
          ok[k] = rdy != 0;
          const int j = ok[k] ? __builtin_ctz(rdy) : 0;
          pend &= ~(rdy & -rdy); rdy &= rdy - 1;
          const int src = ok[k] ? WS_PART + ((kvh * WPH + j) * 32 + b * 4) * PREC : WS_BYTES;
          pa[k] = __builtin_amdgcn_raw_buffer_load_b128(rw, src + g * PREC + c * 32, 0, 16);
          pc[k] = __builtin_amdgcn_raw_buffer_load_b128(rw, src + g * PREC + c * 32 + 16, 0, 16);
          pq[k] = __builtin_amdgcn_raw_buffer_load_b64(rw, src + g * PREC + 512, 0, 16);
        }
#pragma unroll
        for (int k = 0; k < 3; ++k) {
          const float mk = ok[k] ? __uint_as_float(pq[k][0]) : -FLT_MAX;
          const float Mn = fmaxf(M, mk);
          const float a = __builtin_amdgcn_exp2f(M - Mn), wt = __builtin_amdgcn_exp2f(mk - Mn);
          L = L * a + wt * __uint_as_float(pq[k][1]);
          o0 = o0 * a + wt * __builtin_bit_cast(f32x4, pa[k]);
          o1 = o1 * a + wt * __builtin_bit_cast(f32x4, pc[k]);
          M = Mn;
        }
      }
      lds_st<f32x4>(lds, L_EXT + (w * 4 + g) * 512 + c * 32, o0);
      lds_st<f32x4>(lds, L_EXT + (w * 4 + g) * 512 + c * 32 + 16, o1);
      if (c == 0) lds_st<f32x2>(lds, L_ML + ((NWAVE + w) * 32 + g) * 8, f32x2{M, L});
    }
    __syncthreads();
    if (tid < 128) {
      const int row = b * 4 + r4;
      const bool mine = (wgmask >> b) & 1;
      f32x2 ml[12]; f32x4 ow[12];
      float M = -FLT_MAX;
#pragma unroll
      for (int ww = 0; ww < 12; ++ww) {
        if (ww < NWAVE) {
          ml[ww] = mine ? lds_ld<f32x2>(lds, L_ML + (ww * 32 + row) * 8) : f32x2{-FLT_MAX, 0.f};
          ow[ww] = mine ? lds_ld<f32x4>(lds, L_RING + ww * 16384 + row * 512 + ((ch ^ (row & 7)) * 16)) : f32x4{0.f, 0.f, 0.f, 0.f};
        } else {
          ml[ww] = lds_ld<f32x2>(lds, L_ML + (ww * 32 + r4) * 8);
          ow[ww] = lds_ld<f32x4>(lds, L_EXT + ((ww - NWAVE) * 4 + r4) * 512 + ch * 16);
        }
        M = fmaxf(M, ml[ww][0]);
      }
      float L = 0.f; f32x4 o = {0.f, 0.f, 0.f, 0.f};
#pragma unroll
      for (int ww = 0; ww < 12; ++ww) {
        const float wt = __builtin_amdgcn_exp2f(ml[ww][0] - M);
        L = __builtin_fmaf(wt, ml[ww][1], L);
        o += wt * ow[ww];
      }
      int cx = ctx[0];
#pragma unroll
      for (int bb = 1; bb < B; ++bb) if (b == bb) cx = ctx[bb];
      constexpr float MASKED = -998244352.0f * 1.4426950408889634f;
      if (cx < LK && MASKED - M > -200.f) {
        f32x4 sv = {0.f, 0.f, 0.f, 0.f};
        for (int t = cx; t < LK; ++t) {
          int ph = bt[b * NB + (t >> 4)];
          ph = ph < 0 ? ph + (int)nphys : ph;
          const u32x2 vv = *reinterpret_cast<const u32x2*>(vc + (size_t)ph * PAGE_BYTES + (t & 15) * 2048 + kvh * 256 + ch * 8);
          sv += f32x4{__uint_as_float(vv[0] << 16), __uint_as_float(vv[0] & 0xFFFF0000u), __uint_as_float(vv[1] << 16), __uint_as_float(vv[1] & 0xFFFF0000u)};
        }
        const float Mn = fmaxf(M, MASKED);
        const float a = __builtin_amdgcn_exp2f(M - Mn), wt = __builtin_amdgcn_exp2f(MASKED - Mn);
        L = L * a + wt * (float)(LK - cx);
        o = o * a + wt * sv;
      }
      const float inv = 1.f / L;
      *reinterpret_cast<u32x2*>(out + (b * H + kvh * G + r4) * D + ch * 4) = u32x2{cvtpk(o[0] * inv, o[1] * inv), cvtpk(o[2] * inv, o[3] * inv)};
      if (tid == 0) __hip_atomic_store((gu64p)(ws + WS_EPOCH) + kvh * B + b, tb, __ATOMIC_RELAXED, __HIP_MEMORY_SCOPE_AGENT);
    }
    if (om & (om - 1)) __syncthreads();
  }
  STAMP(6);
  STAMP_OUT();
}
}

extern "C" void kernel_launch(void* const* d_in, const int* in_sizes, int n_in, void* d_out, int out_size, void* d_ws, size_t ws_size,
                              hipStream_t stream) {
  attn_decode<<<HKV * WPH, NTHR, 0, stream>>>((const unsigned char*)d_in[0], (const unsigned char*)d_in[1], (const unsigned char*)d_in[2],
                                              (const int*)d_in[3], (const int*)d_in[4], (unsigned char*)d_ws, (unsigned short*)d_out,
                                              (long long)(in_sizes[1] / (PAGE * HKV * D)));
}
```

```cpp
#define PROBE 0
#define PROBE_COND true
#include <hip/hip_runtime.h>
#include <hip/hip_bf16.h>
#include <stdint.h>
#include <float.h>

#define KV_NT 1
#define HEAD_LOW 1
#define W_OWN 6
#define W_SHR 3

namespace {
constexpr int B = 8, H = 32, HKV = 8, G = 4, D = 128, PAGE = 16, NB = 512, LK = NB * PAGE;
constexpr int WPH = 32;
constexpr int NWAVE = 8, NTHR = NWAVE * 64;
constexpr int PAGE_BYTES = PAGE * HKV * D * 2;
constexpr float CSC = 0.088388347648318440f * 1.4426950408889634f;
constexpr int PREC = 132 * 4;
constexpr int L_RING = 0;
constexpr int L_TBL = NWAVE * 16384;
constexpr int L_EXT = L_TBL + B * NB * 4;
constexpr int L_ML = L_EXT + 4 * 4 * 512;
constexpr int L_FM = L_ML + 12 * 32 * 8;
constexpr int L_END = L_FM + 64;
constexpr int WS_EPOCH = 0;
constexpr int WS_FLAG = 4096;
constexpr int WS_PART = WS_FLAG + HKV * B * WPH * 8;
constexpr int WS_BYTES = WS_PART + HKV * WPH * 32 * PREC;
constexpr unsigned long long TAG_STEP = 0x9E3779B97F4A7C15ull;

typedef __attribute__((ext_vector_type(4))) unsigned u32x4;
typedef __attribute__((ext_vector_type(2))) unsigned u32x2;
typedef __attribute__((ext_vector_type(4))) float f32x4;
typedef __attribute__((ext_vector_type(2))) float f32x2;
typedef __attribute__((ext_vector_type(8))) __bf16 bf16x8;
typedef __attribute__((ext_vector_type(4))) short s16x4;
typedef __attribute__((address_space(3))) unsigned char* ldsp;
typedef __attribute__((address_space(1))) unsigned long long* gu64p;

__device__ __forceinline__ unsigned cvtpk(float lo, float hi) {
  typedef __attribute__((ext_vector_type(2))) __bf16 bf16x2;
  bf16x2 r = {(__bf16)lo, (__bf16)hi};
  return __builtin_bit_cast(unsigned, r);
}
__device__ __forceinline__ float xg_max(float x) {
  auto s = __builtin_amdgcn_permlane16_swap(__float_as_uint(x), __float_as_uint(x), false, false);
  x = fmaxf(__uint_as_float(s[0]), __uint_as_float(s[1]));
  auto t = __builtin_amdgcn_permlane32_swap(__float_as_uint(x), __float_as_uint(x), false, false);
  return fmaxf(__uint_as_float(t[0]), __uint_as_float(t[1]));
}
__device__ __forceinline__ float xg_sum(float x) {
  auto s = __builtin_amdgcn_permlane16_swap(__float_as_uint(x), __float_as_uint(x), false, false);
  x = __uint_as_float(s[0]) + __uint_as_float(s[1]);
  auto t = __builtin_amdgcn_permlane32_swap(__float_as_uint(x), __float_as_uint(x), false, false);
  return __uint_as_float(t[0]) + __uint_as_float(t[1]);
}
template <typename T> __device__ __forceinline__ T lds_ld(ldsp p, int off) { return *(__attribute__((address_space(3))) T*)(p + off); }
template <typename T> __device__ __forceinline__ void lds_st(ldsp p, int off, T v) { *(__attribute__((address_space(3))) T*)(p + off) = v; }

#define STAMP(i) do { if (i == PROBE) st_ = __builtin_amdgcn_s_memrealtime(); } while (0)
#define STAMP_OUT() do { if (PROBE_COND) while (__builtin_amdgcn_s_memrealtime() < st_ + 4000) __builtin_amdgcn_s_sleep(2); } while (0)
#define KVPOL " nt"
__device__ __forceinline__ void dma4(const int (&vo)[4], u32x4 rs, unsigned soff, unsigned ldsaddr) {
  asm volatile("s_mov_b32 m0, %6\n\ts_nop 4\n\t"
               "buffer_load_dwordx4 %0, %4, %5 offen" KVPOL " lds\n\t"
               "buffer_load_dwordx4 %1, %4, %5 offen offset:1024" KVPOL " lds\n\t"
               "buffer_load_dwordx4 %2, %4, %5 offen offset:2048" KVPOL " lds\n\t"
               "buffer_load_dwordx4 %3, %4, %5 offen offset:3072" KVPOL " lds"
               :: "v"(vo[0]), "v"(vo[1]), "v"(vo[2]), "v"(vo[3]), "s"(rs), "s"(soff), "s"(ldsaddr) : "memory");
}

__global__ void __launch_bounds__(NTHR, 2)
attn_decode(const unsigned char* q_, const unsigned char* kc_, const unsigned char* vc_,
            const int* bt_, const int* ctxl_, unsigned char* ws_, unsigned short* out_, long long nphys_) {
  unsigned long long st_;
  asm volatile("s_memrealtime %0" : "=s"(st_));
  typedef const __attribute__((address_space(4))) unsigned long long* kap;
  kap ka_ = (kap)__builtin_amdgcn_kernarg_segment_ptr();
  asm volatile("" : "+s"(ka_));
  typedef __attribute__((address_space(1))) unsigned char* gp;
  const unsigned char* q = (const unsigned char*)(gp)ka_[0]; const unsigned char* kc = (const unsigned char*)(gp)ka_[1]; const unsigned char* vc = (const unsigned char*)(gp)ka_[2];
  const int* bt = (const int*)(gp)ka_[3]; const int* __restrict__ ctxl = (const int*)(gp)ka_[4]; unsigned char* ws = (unsigned char*)(gp)ka_[5]; unsigned short* out = (unsigned short*)(gp)ka_[6];
  long long nphys = ka_[7];
  __shared__ __attribute__((aligned(16))) unsigned char smem[L_END];
  const ldsp lds = (ldsp)smem;
  const int tid = threadIdx.x, lane = tid & 63;
  const int w = __builtin_amdgcn_readfirstlane(tid >> 6);
  const int g = lane >> 4, c = lane & 15;
  const int kvh = blockIdx.x & 7, wi = blockIdx.x >> 3;
  int e[B];
#pragma unroll
  for (int b = 0; b < B; ++b) e[b] = bt[b * NB + tid];
  unsigned long long tag = __hip_atomic_load((gu64p)(ws + WS_EPOCH) + kvh * B + (lane & 7), __ATOMIC_RELAXED, __HIP_MEMORY_SCOPE_AGENT) + TAG_STEP;
  u32x4 qf[2][4];
#pragma unroll
  for (int x = 0; x < 2; ++x)
#pragma unroll
    for (int kk = 0; kk < 4; ++kk)
      qf[x][kk] = *reinterpret_cast<const u32x4*>(q + (((x * 4 + (c >> 2)) * H + kvh * G + (c & 3)) * D + kk * 32 + g * 8) * 2);
  int limv[2] = {ctxl[c >> 2], ctxl[4 + (c >> 2)]};
  int ctx[B], np[B], npmax = 0;
#pragma unroll
  for (int b = 0; b < B; ++b) { int t = ctxl[b]; t = t < 0 ? 0 : (t > LK ? LK : t); ctx[b] = t; np[b] = (t + PAGE - 1) / PAGE; npmax = max(npmax, np[b]); }
  bool mis = false;
#pragma unroll
  for (int b = 0; b < B; ++b) { lds_st<int>(lds, L_TBL + (b * NB + tid) * 4, e[b]); mis |= (e[b] != e[0]); }
  {
    const unsigned long long bal = __ballot(mis);
    const int first = bal ? (w * 64 + __builtin_ctzll(bal)) : NB;
    if (lane == 0) lds_st<int>(lds, L_FM + w * 4, first);
  }
  __syncthreads();
  STAMP(1);
  int S;
  {
    const u32x4 f0 = lds_ld<u32x4>(lds, L_FM), f1 = lds_ld<u32x4>(lds, L_FM + 16);
    const unsigned s = min(min(min(f0[0], f0[1]), min(f0[2], f0[3])), min(min(f1[0], f1[1]), min(f1[2], f1[3])));
    S = min(__builtin_amdgcn_readfirstlane((int)s), npmax);
  }
  int off[B + 1];
  off[0] = S;
#pragma unroll
  for (int b = 0; b < B; ++b) off[b + 1] = off[b] + max(0, np[b] - S);
  const int Wp = off[B];
  int cj, ownmask;
  {
    int mid = off[0] + off[1];
#pragma unroll
    for (int b = 1; b < B; ++b) mid = (lane & 7) == b ? off[b] + off[b + 1] : mid;
    const float rw = 16.f * __builtin_amdgcn_rcpf((float)max(Wp, 1));
    const int own = min((int)((float)mid * rw), WPH - 1);
    ownmask = (int)(__ballot(own == wi) & 0xFF);
    unsigned cm = 0;
#pragma unroll
    for (int b = 0; b < B; ++b) cm |= 1u << __builtin_amdgcn_readlane(own, b);
    const int nsh = (int)((float)(2 * S) * rw);
    const int j = min(lane, WPH);
    const int cum = 64 * j + W_OWN * __builtin_popcount(cm & (unsigned)((1ull << j) - 1)) - W_SHR * min(j, nsh);
    const int tot = __builtin_amdgcn_readlane(cum, WPH);
    cj = min((int)((float)(Wp * cum) * __builtin_amdgcn_rcpf((float)tot)), Wp);
    cj = lane >= WPH ? Wp : cj;
  }
  const int c0 = __builtin_amdgcn_readlane(cj, wi), c1 = __builtin_amdgcn_readlane(cj, wi + 1);
  const int n = (c1 - c0 - w + NWAVE - 1) >> 3;
  unsigned dphys; int djb;
  {
    const int f = max(min(c0 + w + NWAVE * lane, Wp - 1), 0);
    int idx = f;
#pragma unroll
    for (int b = 0; b < B; ++b) idx = f >= off[b] ? f + (S + b * NB - off[b]) : idx;
    int ph = lds_ld<int>(lds, L_TBL + idx * 4);
    ph = ph < 0 ? ph + (int)nphys : ph;
    dphys = (unsigned)ph * PAGE_BYTES;
    djb = ((idx & (NB - 1)) * PAGE) | (f < S ? 0xFF0000 : 0x10000 << (idx >> 9));
  }
  int kvo[4], vvo[4];
#pragma unroll
  for (int t = 0; t < 4; ++t) {
    const int row = 4 * t + g;
    kvo[t] = row * 2048 + ((c ^ row) * 16) + kvh * 256 - t * 1024;
    vvo[t] = row * 2048 + ((c ^ ((row & 7) << 1)) * 16) + kvh * 256 - t * 1024;
  }
  const unsigned cbytes = (unsigned)nphys * PAGE_BYTES;
  u32x4 rk = {(unsigned)(uintptr_t)kc, (unsigned)((uintptr_t)kc >> 32), 0, 0x00020000u};
  u32x4 rv = {(unsigned)(uintptr_t)vc, (unsigned)((uintptr_t)vc >> 32), 0, 0x00020000u};
  const unsigned ring = (unsigned)(uintptr_t)lds + L_RING + w * 16384;
  asm volatile("" : "+v"(qf[0][0]), "+v"(qf[0][1]), "+v"(qf[0][2]), "+v"(qf[0][3]), "+v"(qf[1][0]), "+v"(qf[1][1]), "+v"(qf[1][2]), "+v"(qf[1][3]),
               "+v"(tag), "+v"(limv[0]), "+v"(limv[1]));
#pragma unroll
  for (int u = 0; u < 2; ++u) {
    const unsigned so = __builtin_amdgcn_readlane(dphys, u);
    rk[2] = rv[2] = u < n ? cbytes : 0;
    dma4(kvo, rk, so, ring + u * 8192);
    dma4(vvo, rv, so, ring + u * 8192 + 4096);
  }
  STAMP(2);
  int wgmask = (c0 < S && c0 < c1) ? 0xFF : 0;
#pragma unroll
  for (int b = 0; b < B; ++b) if (max(c0, off[b]) < min(c1, off[b + 1])) wgmask |= 1 << b;
  int bbit[2];
#pragma unroll
  for (int x = 0; x < 2; ++x) {
    limv[x] = min(max(limv[x], 0), LK) - 4 * g;
    bbit[x] = 1 << (16 + 4 * x + (c >> 2));
  }
  int ka = L_RING + w * 16384 + c * 256 + ((g ^ c) * 16);
  int va = L_RING + w * 16384 + 4096 + (4 * g + (c >> 2)) * 256 + ((((4 * g + (c >> 2)) & 7) << 5) | ((c & 3) << 3));

  f32x4 acc[2][8];
  float m[2], l[2];
#pragma unroll
  for (int x = 0; x < 2; ++x) { m[x] = -FLT_MAX; l[x] = 0.f;
#pragma unroll
    for (int d = 0; d < 8; ++d) acc[x][d] = f32x4{0.f, 0.f, 0.f, 0.f}; }

  unsigned slot = ring;
  for (int i = 0; i < n; ++i) {
    const int jb = __builtin_amdgcn_readlane(djb, i);
    const unsigned so = __builtin_amdgcn_readlane(dphys, i + 2);
    rk[2] = rv[2] = i + 2 < n ? cbytes : 0;
    asm volatile("s_waitcnt vmcnt(12)" ::: "memory");
    if (i == 0) STAMP(3);
    u32x4 kf[4];
#pragma unroll
    for (int kk = 0; kk < 4; ++kk) kf[kk] = lds_ld<u32x4>(lds, ka ^ (kk * 64));
    asm volatile("s_waitcnt lgkmcnt(0)" : "+v"(kf[0]), "+v"(kf[1]), "+v"(kf[2]), "+v"(kf[3]) :: "memory");
    dma4(kvo, rk, so, slot);
    u32x2 pb[2];
#pragma unroll
    for (int x = 0; x < 2; ++x) {
      if (jb & (0xF0000 << (4 * x))) {
        f32x4 s = {0.f, 0.f, 0.f, 0.f};
#pragma unroll
        for (int kk = 0; kk < 4; ++kk)
          s = __builtin_amdgcn_mfma_f32_16x16x32_bf16(__builtin_bit_cast(bf16x8, kf[kk]), __builtin_bit_cast(bf16x8, qf[x][kk]), s, 0, 0, 0);
        const int lim = (jb & bbit[x]) ? limv[x] - (jb & 0xFFFF) : 0;
#pragma unroll
        for (int r = 0; r < 4; ++r) s[r] = (r < lim) ? s[r] : -INFINITY;
        float mx = fmaxf(fmaxf(s[0], s[1]), fmaxf(s[2], s[3]));
        mx = xg_max(mx) * CSC;
        const float mn = fmaxf(m[x], mx);
        const float al = __builtin_amdgcn_exp2f(m[x] - mn);
        m[x] = mn;
        float p[4];
#pragma unroll
        for (int r = 0; r < 4; ++r) p[r] = __builtin_amdgcn_exp2f(__builtin_fmaf(s[r], CSC, -mn));
        l[x] = __builtin_fmaf(l[x], al, (p[0] + p[1]) + (p[2] + p[3]));
        pb[x] = u32x2{cvtpk(p[0], p[1]), cvtpk(p[2], p[3])};
#pragma unroll
        for (int d = 0; d < 8; ++d) acc[x][d] *= al;
      }
    }
    asm volatile("s_waitcnt vmcnt(12)" ::: "memory");
    s16x4 vt[8];
#pragma unroll
    for (int d = 0; d < 8; ++d) {
      typedef __attribute__((ext_vector_type(4))) short v4i16;
      vt[d] = __builtin_amdgcn_ds_read_tr16_b64_v4i16((__attribute__((address_space(3))) v4i16*)(lds + (va ^ (d * 32))));
    }
    asm volatile("s_waitcnt lgkmcnt(0)" : "+v"(vt[0]), "+v"(vt[1]), "+v"(vt[2]), "+v"(vt[3]), "+v"(vt[4]), "+v"(vt[5]), "+v"(vt[6]), "+v"(vt[7]) :: "memory");
    dma4(vvo, rv, so, slot + 4096);
#pragma unroll
    for (int x = 0; x < 2; ++x) {
      if (jb & (0xF0000 << (4 * x))) {
#pragma unroll
        for (int d = 0; d < 8; ++d)
          acc[x][d] = __builtin_amdgcn_mfma_f32_16x16x16bf16_1k(vt[d], __builtin_bit_cast(s16x4, pb[x]), acc[x][d], 0, 0, 0);
      }
    }
    slot ^= 8192; ka ^= 8192; va ^= 8192;
  }
  asm volatile("s_waitcnt vmcnt(0)" ::: "memory");
  STAMP(4);
#pragma unroll
  for (int x = 0; x < 2; ++x) {
    if (wgmask & (0xF << (4 * x))) {
      const float lt = xg_sum(l[x]);
      if ((wgmask << 16) & bbit[x]) {
        const int rb = L_RING + w * 16384 + (x * 16 + c) * 512;
#pragma unroll
        for (int d = 0; d < 8; ++d) lds_st<f32x4>(lds, rb + (((4 * d + g) ^ (c & 7)) * 16), acc[x][d]);
        if (g == 0) lds_st<f32x2>(lds, L_ML + (w * 32 + x * 16 + c) * 8, f32x2{m[x], lt});
      }
    }
  }
  const auto rw = __builtin_amdgcn_make_buffer_rsrc(ws, 0, WS_BYTES, 0x00020000);
  const int r4 = (tid >> 5) & 3, ch = tid & 31;
  int pub = wgmask & ~ownmask;
  if (pub) {
    __syncthreads();
    int pm = pub;
    for (int k = w >> 1; k > 0; --k) pm &= pm - 1;
    while (pm) {
      const int row = __builtin_ctz(pm) * 4 + r4;
      pm &= pm - 1; pm &= pm - 1; pm &= pm - 1; pm &= pm - 1;
      f32x2 ml[NWAVE]; f32x4 ow[NWAVE];
      float M = -FLT_MAX;
#pragma unroll
      for (int ww = 0; ww < NWAVE; ++ww) {
        ml[ww] = lds_ld<f32x2>(lds, L_ML + (ww * 32 + row) * 8);
        ow[ww] = lds_ld<f32x4>(lds, L_RING + ww * 16384 + row * 512 + ((ch ^ (row & 7)) * 16));
        M = fmaxf(M, ml[ww][0]);
      }
      float L = 0.f; f32x4 o = {0.f, 0.f, 0.f, 0.f};
#pragma unroll
      for (int ww = 0; ww < NWAVE; ++ww) {
        const float wt = __builtin_amdgcn_exp2f(ml[ww][0] - M);
        L = __builtin_fmaf(wt, ml[ww][1], L);
        o += wt * ow[ww];
      }
      const int dst = WS_PART + ((kvh * WPH + wi) * 32 + row) * PREC;
      __builtin_amdgcn_raw_buffer_store_b128(__builtin_bit_cast(u32x4, o), rw, dst + ch * 16, 0, 16);
      if (ch == 0) __builtin_amdgcn_raw_buffer_store_b64(u32x2{__float_as_uint(M), __float_as_uint(L)}, rw, dst + 512, 0, 16);
    }
    asm volatile("s_waitcnt vmcnt(0)" ::: "memory");
    __syncthreads();
    if (tid < B && ((pub >> tid) & 1))
      __hip_atomic_store((gu64p)(ws + WS_FLAG) + (kvh * B + tid) * WPH + wi, tag, __ATOMIC_RELAXED, __HIP_MEMORY_SCOPE_AGENT);
  }
  STAMP(5);
  for (int om = ownmask; om; om &= om - 1) {
    const int b = __builtin_ctz(om);
    const unsigned long long tb = ((unsigned long long)(unsigned)__builtin_amdgcn_readlane((unsigned)(tag >> 32), b) << 32) | (unsigned)__builtin_amdgcn_readlane((unsigned)tag, b);
    if (w < 4) {
      int ob = off[0], oe = off[1];
#pragma unroll
      for (int bb = 1; bb < B; ++bb) if (b == bb) { ob = off[bb]; oe = off[bb + 1]; }
      const int cn = __builtin_amdgcn_ds_bpermute(min(lane + 1, 63) * 4, cj);
      const bool con = (cj < S && cj < cn) || max(cj, ob) < min(cn, oe);
      unsigned pend = (unsigned)__ballot(con && lane < WPH && lane != wi && (lane & 3) == w);
      float M = -FLT_MAX, L = 0.f; f32x4 o0 = {0.f, 0.f, 0.f, 0.f}, o1 = o0;
      for (int spin = 0; pend && spin < (1 << 18); ++spin) {
        const unsigned long long fl = __hip_atomic_load((gu64p)(ws + WS_FLAG) + (kvh * B + b) * WPH + (lane & 31), __ATOMIC_RELAXED, __HIP_MEMORY_SCOPE_AGENT);
        unsigned rdy = (unsigned)__ballot(fl == tb) & pend;
        if (!rdy) { __builtin_amdgcn_s_sleep(8); continue; }
        asm volatile("" ::: "memory");
        u32x4 pa[3], pc[3]; u32x2 pq[3]; bool ok[3];
#pragma unroll
        for (int k = 0; k < 3; ++k) {
          ok[k] = rdy != 0;
          const int j = ok[k] ? __builtin_ctz(rdy) : 0;
          pend &= ~(rdy & -rdy); rdy &= rdy - 1;
          const int src = ok[k] ? WS_PART + ((kvh * WPH + j) * 32 + b * 4) * PREC : WS_BYTES;
          pa[k] = __builtin_amdgcn_raw_buffer_load_b128(rw, src + g * PREC + c * 32, 0, 16);
          pc[k] = __builtin_amdgcn_raw_buffer_load_b128(rw, src + g * PREC + c * 32 + 16, 0, 16);
          pq[k] = __builtin_amdgcn_raw_buffer_load_b64(rw, src + g * PREC + 512, 0, 16);
        }
#pragma unroll
        for (int k = 0; k < 3; ++k) {
          const float mk = ok[k] ? __uint_as_float(pq[k][0]) : -FLT_MAX;
          const float Mn = fmaxf(M, mk);
          const float a = __builtin_amdgcn_exp2f(M - Mn), wt = __builtin_amdgcn_exp2f(mk - Mn);
          L = L * a + wt * __uint_as_float(pq[k][1]);
          o0 = o0 * a + wt * __builtin_bit_cast(f32x4, pa[k]);
          o1 = o1 * a + wt * __builtin_bit_cast(f32x4, pc[k]);
          M = Mn;
        }
      }
      lds_st<f32x4>(lds, L_EXT + (w * 4 + g) * 512 + c * 32, o0);
      lds_st<f32x4>(lds, L_EXT + (w * 4 + g) * 512 + c * 32 + 16, o1);
      if (c == 0) lds_st<f32x2>(lds, L_ML + ((NWAVE + w) * 32 + g) * 8, f32x2{M, L});
    }
    __syncthreads();
    if (tid < 128) {
      const int row = b * 4 + r4;
      const bool mine = (wgmask >> b) & 1;
      f32x2 ml[12]; f32x4 ow[12];
      float M = -FLT_MAX;
#pragma unroll
      for (int ww = 0; ww < 12; ++ww) {
        if (ww < NWAVE) {
          ml[ww] = mine ? lds_ld<f32x2>(lds, L_ML + (ww * 32 + row) * 8) : f32x2{-FLT_MAX, 0.f};
          ow[ww] = mine ? lds_ld<f32x4>(lds, L_RING + ww * 16384 + row * 512 + ((ch ^ (row & 7)) * 16)) : f32x4{0.f, 0.f, 0.f, 0.f};
        } else {
          ml[ww] = lds_ld<f32x2>(lds, L_ML + (ww * 32 + r4) * 8);
          ow[ww] = lds_ld<f32x4>(lds, L_EXT + ((ww - NWAVE) * 4 + r4) * 512 + ch * 16);
        }
        M = fmaxf(M, ml[ww][0]);
      }
      float L = 0.f; f32x4 o = {0.f, 0.f, 0.f, 0.f};
#pragma unroll
      for (int ww = 0; ww < 12; ++ww) {
        const float wt = __builtin_amdgcn_exp2f(ml[ww][0] - M);
        L = __builtin_fmaf(wt, ml[ww][1], L);
        o += wt * ow[ww];
      }
      int cx = ctx[0];
#pragma unroll
      for (int bb = 1; bb < B; ++bb) if (b == bb) cx = ctx[bb];
      constexpr float MASKED = -998244352.0f * 1.4426950408889634f;
      if (cx < LK && MASKED - M > -200.f) {
        f32x4 sv = {0.f, 0.f, 0.f, 0.f};
        for (int t = cx; t < LK; ++t) {
          int ph = bt[b * NB + (t >> 4)];
          ph = ph < 0 ? ph + (int)nphys : ph;
          const u32x2 vv = *reinterpret_cast<const u32x2*>(vc + (size_t)ph * PAGE_BYTES + (t & 15) * 2048 + kvh * 256 + ch * 8);
          sv += f32x4{__uint_as_float(vv[0] << 16), __uint_as_float(vv[0] & 0xFFFF0000u), __uint_as_float(vv[1] << 16), __uint_as_float(vv[1] & 0xFFFF0000u)};
        }
        const float Mn = fmaxf(M, MASKED);
        const float a = __builtin_amdgcn_exp2f(M - Mn), wt = __builtin_amdgcn_exp2f(MASKED - Mn);
        L = L * a + wt * (float)(LK - cx);
        o = o * a + wt * sv;
      }
      const float inv = 1.f / L;
      *reinterpret_cast<u32x2*>(out + (b * H + kvh * G + r4) * D + ch * 4) = u32x2{cvtpk(o[0] * inv, o[1] * inv), cvtpk(o[2] * inv, o[3] * inv)};
      if (tid == 0) __hip_atomic_store((gu64p)(ws + WS_EPOCH) + kvh * B + b, tb, __ATOMIC_RELAXED, __HIP_MEMORY_SCOPE_AGENT);
    }
    if (om & (om - 1)) __syncthreads();
  }
  STAMP(6);
  STAMP_OUT();
}
}

extern "C" void kernel_launch(void* const* d_in, const int* in_sizes, int n_in, void* d_out, int out_size, void* d_ws, size_t ws_size,
                              hipStream_t stream) {
  attn_decode<<<HKV * WPH, NTHR, 0, stream>>>((const unsigned char*)d_in[0], (const unsigned char*)d_in[1], (const unsigned char*)d_in[2],
                                              (const int*)d_in[3], (const int*)d_in[4], (unsigned char*)d_ws, (unsigned short*)d_out,
                                              (long long)(in_sizes[1] / (PAGE * HKV * D)));
}
```

```cpp
#include <hip/hip_runtime.h>
#include <hip/hip_bf16.h>
#include <stdint.h>
#include <float.h>

#define NSLOW 38
#define NS 2
#define SHR_A 8
#define OWN_B 0
#define WARM 1
#define LAZY 16

namespace {
constexpr int B = 8, H = 32, HKV = 8, G = 4, D = 128, PAGE = 16, NB = 512, LK = NB * PAGE;
constexpr int NFAST = (128 - NSLOW) / 3;
static_assert(NSLOW + 3 * NFAST == 128 && NSLOW < 64, "");
constexpr int NWAVE = 8, NTHR = NWAVE * 64, NSTR = 4;
constexpr int PAGE_BYTES = PAGE * HKV * D * 2;
constexpr float CSC = 0.088388347648318440f * 1.4426950408889634f;
constexpr int GK = 6;
constexpr int ROW = 528, RECW = 32 * ROW;
constexpr int L_TBL = 0;
constexpr int L_Q = L_TBL + B * NB * 4;
constexpr int L_ML = L_Q + 32 * 256;
constexpr int L_FM = L_ML + 32 * 32;
constexpr int L_CNT = L_FM + 32;
constexpr int L_RING = NS == 2 ? 32768 - 4096 : 32768;
constexpr int RINGW = NS * 8192;
constexpr int L_REC = NS == 2 ? L_RING + NSTR * RINGW : L_RING;
constexpr int RECS = NS == 2 ? RECW : RINGW;
constexpr int L_END = L_REC + NSTR * RECS;
static_assert(L_CNT + 4 <= L_RING && L_END <= 163840, "");
constexpr int WS_EPOCH = 0;
constexpr int AREA = B * 4096 + 1024;
constexpr int WS_PART = 4096;
constexpr int WS_BYTES = WS_PART + 256 * AREA;
constexpr unsigned long long TAG_STEP = 0x9E3779B97F4A7C15ull;

typedef __attribute__((ext_vector_type(4))) unsigned u32x4;
typedef __attribute__((ext_vector_type(2))) unsigned u32x2;
typedef __attribute__((ext_vector_type(4))) float f32x4;
typedef __attribute__((ext_vector_type(2))) float f32x2;
typedef __attribute__((ext_vector_type(8))) __bf16 bf16x8;
typedef __attribute__((ext_vector_type(4))) short s16x4;
typedef __attribute__((address_space(3))) unsigned char* ldsp;
typedef __attribute__((address_space(1))) unsigned long long* gu64p;

__device__ __forceinline__ unsigned cvtpk(float lo, float hi) {
  typedef __attribute__((ext_vector_type(2))) __bf16 bf16x2;
  bf16x2 r = {(__bf16)lo, (__bf16)hi};
  return __builtin_bit_cast(unsigned, r);
}
__device__ __forceinline__ float xg_max(float x) {
  auto s = __builtin_amdgcn_permlane16_swap(__float_as_uint(x), __float_as_uint(x), false, false);
  x = fmaxf(__uint_as_float(s[0]), __uint_as_float(s[1]));
  auto t = __builtin_amdgcn_permlane32_swap(__float_as_uint(x), __float_as_uint(x), false, false);
  return fmaxf(__uint_as_float(t[0]), __uint_as_float(t[1]));
}
__device__ __forceinline__ float xg_sum(float x) {
  auto s = __builtin_amdgcn_permlane16_swap(__float_as_uint(x), __float_as_uint(x), false, false);
  x = __uint_as_float(s[0]) + __uint_as_float(s[1]);
  auto t = __builtin_amdgcn_permlane32_swap(__float_as_uint(x), __float_as_uint(x), false, false);
  return __uint_as_float(t[0]) + __uint_as_float(t[1]);
}
template <int SH> __device__ __forceinline__ int row_shr(int x) { return __builtin_amdgcn_update_dpp(0, x, 0x110 + SH, 0xF, 0xF, true); }
template <int CTL> __device__ __forceinline__ float dpp(float x) { return __int_as_float(__builtin_amdgcn_update_dpp(0, __float_as_int(x), CTL, 0xF, 0xF, true)); }
#define DPP2(op, a, b) "s_nop 1\n\t" op " %0, %0, %0 " a " row_mask:0xf bank_mask:0xf\n\ts_nop 1\n\t" op " %0, %0, %0 " b " row_mask:0xf bank_mask:0xf\n\t"
__device__ __forceinline__ float quad_max(float x) { asm(DPP2("v_max_f32_dpp", "quad_perm:[1,0,3,2]", "quad_perm:[2,3,0,1]") : "+v"(x)); return x; }
__device__ __forceinline__ float quad_sum(float x) { asm(DPP2("v_add_f32_dpp", "quad_perm:[1,0,3,2]", "quad_perm:[2,3,0,1]") : "+v"(x)); return x; }
__device__ __forceinline__ float row_max(float x) { asm(DPP2("v_max_f32_dpp", "row_ror:8", "row_ror:4") DPP2("v_max_f32_dpp", "row_ror:2", "row_ror:1") : "+v"(x)); return x; }
__device__ __forceinline__ float row_sum(float x) { asm(DPP2("v_add_f32_dpp", "row_ror:8", "row_ror:4") DPP2("v_add_f32_dpp", "row_ror:2", "row_ror:1") : "+v"(x)); return x; }
__device__ __forceinline__ void rec_reads(int& done, f32x2& ml, f32x2 (&xv)[NSTR], int a_cnt, int a_ml, int a_x) {
  asm volatile("ds_read_b32 %0, %6\n\tds_read_b64 %1, %7\n\tds_read_b64 %2, %8\n\tds_read_b64 %3, %8 offset:%9\n\tds_read_b64 %4, %8 offset:%10\n\tds_read_b64 %5, %8 offset:%11\n\ts_waitcnt lgkmcnt(0)"
               : "=&v"(done), "=&v"(ml), "=&v"(xv[0]), "=&v"(xv[1]), "=&v"(xv[2]), "=&v"(xv[3]) : "v"(a_cnt), "v"(a_ml), "v"(a_x), "i"(RECS), "i"(2 * RECS), "i"(3 * RECS) : "memory");
}
constexpr int ALL_DONE = 0x01010101;
__device__ __forceinline__ float rdl(float x, int l) { return __int_as_float(__builtin_amdgcn_readlane(__float_as_int(x), l)); }
template <typename T> __device__ __forceinline__ T lds_ld(ldsp p, int off) { return *(__attribute__((address_space(3))) T*)(p + off); }
template <typename T> __device__ __forceinline__ T lds_vld(ldsp p, int off) { return *(volatile __attribute__((address_space(3))) T*)(p + off); }
template <typename T> __device__ __forceinline__ void lds_st(ldsp p, int off, T v) { *(__attribute__((address_space(3))) T*)(p + off) = v; }
template <typename T> __device__ __forceinline__ void lds_vst(ldsp p, int off, T v) { *(volatile __attribute__((address_space(3))) T*)(p + off) = v; }
__device__ __forceinline__ u32x4 seal(f32x2 v, unsigned tl, unsigned th) {
  return u32x4{__float_as_uint(v[0]), __float_as_uint(v[1]), __float_as_uint(v[0]) ^ tl, __float_as_uint(v[1]) ^ th};
}
__device__ __forceinline__ unsigned stale(u32x4 g, unsigned tl, unsigned th) { return (g[0] ^ g[2] ^ tl) | (g[1] ^ g[3] ^ th); }

#define STAMP(i)
#define STAMP_OUT()
__device__ __forceinline__ void dma4(const int (&vo)[4], u32x4 rs, unsigned soff, unsigned ldsaddr) {
  asm volatile("s_mov_b32 m0, %6\n\ts_nop 4\n\t"
               "buffer_load_dwordx4 %0, %4, %5 offen nt lds\n\t"
               "buffer_load_dwordx4 %1, %4, %5 offen offset:1024 nt lds\n\t"
               "buffer_load_dwordx4 %2, %4, %5 offen offset:2048 nt lds\n\t"
               "buffer_load_dwordx4 %3, %4, %5 offen offset:3072 nt lds"
               :: "v"(vo[0]), "v"(vo[1]), "v"(vo[2]), "v"(vo[3]), "s"(rs), "s"(soff), "s"(ldsaddr) : "memory");
}

__global__ void __launch_bounds__(NTHR, 2)
attn_decode(const unsigned char* __restrict__ q, const unsigned char* kc, const unsigned char* vc,
            const int* __restrict__ bt, const int* __restrict__ ctxl, unsigned char* ws, unsigned short* out, long long nphys) {
  __shared__ __attribute__((aligned(16))) unsigned char smem[L_END];
  const ldsp lds = (ldsp)smem;
  STAMP(0);
  const int tid = threadIdx.x, lane = tid & 63;
  const int w = __builtin_amdgcn_readfirstlane(tid >> 6);
  const int g = lane >> 4, c = lane & 15;
  const int bx = blockIdx.x & 127, h4 = (bx >= NFAST) + (bx >= NFAST + NSLOW) + (bx >= 2 * NFAST + NSLOW);
  const int kvh = (blockIdx.x >> 7) * 4 + h4, nw = h4 == 1 ? NSLOW : NFAST;
  int wi = bx - h4 * NFAST - (h4 >= 2 ? NSLOW - NFAST : 0), rnw = h4 == 1 ? (65536 + NSLOW - 1) / NSLOW : (65536 + NFAST - 1) / NFAST;
  asm volatile("" : "+s"(kc), "+s"(vc), "+s"(out), "+s"(nphys), "+s"(wi), "+s"(rnw));
  if (w < NSTR) __builtin_amdgcn_s_setprio(3);
  int cxv = ctxl[lane & 7];
  int limv[2] = {ctxl[c >> 2], ctxl[4 + (c >> 2)]};
  int e[B];
#pragma unroll
  for (int b = 0; b < B; ++b) e[b] = bt[b * NB + tid];
  const u32x4 qv = *reinterpret_cast<const u32x4*>(q + (w * H + kvh * G) * D * 2 + lane * 16);
  unsigned long long tag = __hip_atomic_load((gu64p)(ws + WS_EPOCH) + kvh * B + ((lane >> 2) & 7), __ATOMIC_RELAXED, __HIP_MEMORY_SCOPE_AGENT);
  int kvo[4], vvo[4];
#pragma unroll
  for (int t = 0; t < 4; ++t) {
    const int row = 4 * t + g;
    kvo[t] = row * 2048 + ((c ^ row) * 16) + kvh * 256 - t * 1024;
    vvo[t] = row * 2048 + ((c ^ ((row & 7) << 1)) * 16) + kvh * 256 - t * 1024;
  }
  int fw = w + NSTR * lane;
  asm volatile("" : "+v"(kvo[0]), "+v"(kvo[1]), "+v"(kvo[2]), "+v"(kvo[3]), "+v"(vvo[0]), "+v"(vvo[1]), "+v"(vvo[2]), "+v"(vvo[3]), "+v"(fw));
  cxv = min(max(cxv, 0), LK);
#pragma unroll
  for (int x = 0; x < 2; ++x) limv[x] = min(max(limv[x], 0), LK) - 4 * g;
  asm volatile("" : "+v"(limv[0]), "+v"(limv[1]));
  int npv = (cxv + PAGE - 1) / PAGE;
  int npmax;
  {
    int t = max(npv, row_shr<1>(npv)); t = max(t, row_shr<2>(t)); t = max(t, row_shr<4>(t));
    npmax = __builtin_amdgcn_readlane(t, 7);
  }
  bool mis = false;
#pragma unroll
  for (int b = 0; b < B; ++b) { lds_st<int>(lds, L_TBL + (b * NB + tid) * 4, e[b]); mis |= (e[b] != e[0]); }
  {
    const unsigned long long bal = __ballot(mis);
    int first = min(bal ? (w * 64 + __builtin_ctzll(bal)) : NB, npmax);
    first = npmax ? first : 1;
    if (lane < 2) lds_st<int>(lds, lane ? L_CNT : L_FM + w * 4, lane ? 0 : first);
  }
  lds_st<u32x4>(lds, L_Q + (4 * w + g) * 256 + ((c ^ ((4 * w + g) & 15)) * 16), qv);
  tag += TAG_STEP;
  unsigned tl = (unsigned)tag, th = (unsigned)(tag >> 32);
  asm volatile("" : "+v"(tl), "+v"(th));
  __syncthreads();
  STAMP(1);
  int S;
  {
    const u32x4 f0 = lds_ld<u32x4>(lds, L_FM), f1 = lds_ld<u32x4>(lds, L_FM + 16);
    const unsigned s = min(min(min(f0[0], f0[1]), min(f0[2], f0[3])), min(min(f1[0], f1[1]), min(f1[2], f1[3])));
    S = __builtin_amdgcn_readfirstlane((int)s);
  }
  const int cnt = max(npv - S, 0);
  int se = cnt + row_shr<1>(cnt); se += row_shr<2>(se); se += row_shr<4>(se); se += S;
  const int sb = se - cnt;
  const int Wp = __builtin_amdgcn_readlane(se, 7);
  const int beta = (Wp * OWN_B) >> 12, ash = S * (SHR_A << 8);
  const int wr = ((((Wp - B * beta) << 6) + (ash >> 10)) * rnw) >> 6;
  const int mid = cnt ? sb + (cnt >> 1) : ((2 * (lane & 7) + 1) * Wp) >> 4;
  const int ymid = max(((mid - (lane & 7) * beta) << 16) + ash, 0);
  const int y0 = wi * wr, y1 = wi + 1 == nw ? 0x7FFFFFFF : y0 + wr;
  const int m0 = (int)(__ballot(ymid < y0) & 0xFF), m1 = (int)(__ballot(ymid < y1) & 0xFF);
  const int c0 = min(((y0 - min((y0 >> 8) * SHR_A, ash)) >> 16) + beta * __builtin_popcount(m0), Wp);
  const int c1 = min(((y1 - min((y1 >> 8) * SHR_A, ash)) >> 16) + beta * __builtin_popcount(m1), Wp);
  const int ownmask = m1 & ~m0;
  const int priv = (int)(__ballot(max(sb, c0) < min(se, c1)) & 0xFF);
  const int wgmask = (c0 < S && c0 < c1) ? 0xFF : priv;
  const auto rw = __builtin_amdgcn_make_buffer_rsrc(ws, 0, WS_BYTES, 0x00020000);
  const int n = (c1 - c0 - w + NSTR - 1) >> 2;
  if (w < NSTR) {
    unsigned dphys; int djb;
    {
      const int f = min(c0 + fw, Wp - 1);
      const int dl = S + (lane & 7) * NB - sb;
      int idx = f;
      for (int pm = priv; pm; pm &= pm - 1) {
        const int b = __builtin_ctz(pm);
        const int s0 = __builtin_amdgcn_readlane(sb, b), d0 = __builtin_amdgcn_readlane(dl, b);
        idx = f >= s0 ? f + d0 : idx;
      }
      int ph = lds_ld<int>(lds, L_TBL + (idx & (B * NB - 1)) * 4);
      ph = ph < 0 ? ph + (int)nphys : ph;
      dphys = (unsigned)ph * PAGE_BYTES;
      djb = ((idx & (NB - 1)) * PAGE) | (f < S ? 0xFF0000 : 0x10000 << ((idx >> 9) & 7));
    }
    const unsigned cbytes = (unsigned)nphys * PAGE_BYTES;
    const u32x4 rk = {(unsigned)(uintptr_t)kc, (unsigned)((uintptr_t)kc >> 32), cbytes, 0x00020000u};
    const u32x4 rv = {(unsigned)(uintptr_t)vc, (unsigned)((uintptr_t)vc >> 32), cbytes, 0x00020000u};
    const unsigned ring = (unsigned)(uintptr_t)lds + L_RING + w * RINGW;
#pragma unroll
    for (int u = 0; u < NS; ++u) {
      if (u >= n) break;
      const unsigned so = __builtin_amdgcn_readlane(dphys, u);
      dma4(kvo, rk, so, ring + u * 8192);
      dma4(vvo, rv, so, ring + u * 8192 + 4096);
    }
    STAMP(2);
    u32x4 qf[2][4];
#pragma unroll
    for (int x = 0; x < 2; ++x)
#pragma unroll
      for (int kk = 0; kk < 4; ++kk) qf[x][kk] = lds_ld<u32x4>(lds, L_Q + (x * 16 + c) * 256 + (((4 * kk + g) ^ c) * 16));
    int bbit[2];
#pragma unroll
    for (int x = 0; x < 2; ++x) bbit[x] = 1 << (16 + 4 * x + (c >> 2));
    int ka[4], va[8];
#pragma unroll
    for (int kk = 0; kk < 4; ++kk) ka[kk] = L_RING + w * RINGW + c * 256 + (((4 * kk + g) ^ c) * 16);
#pragma unroll
    for (int d = 0; d < 8; ++d) va[d] = L_RING + w * RINGW + 4096 + (4 * g + (c >> 2)) * 256 + ((((4 * g + (c >> 2)) & 7) ^ d) << 5) + ((c & 3) << 3);
    const int pub = wgmask & ~ownmask;
    const int lb = (int)(uintptr_t)lds;
    int b0 = __builtin_ctz(pub | 0x80);
    int a_rec = L_REC + w * RECS + c * ROW + g * 16, a_mlw = L_ML + c * 32 + w * 8, a_dn = L_CNT + w, one = 1;
    int a_cnt = lb + L_CNT, a_mlr = lb + L_ML + (((lane >> 2) & 7) * 4 + w) * 32 + (lane & 3) * 8, a_rd = L_REC + w * ROW + lane * 8, a_rd0 = lb + a_rd + b0 * 4 * ROW;
    int o_ml = (lane < 32 && (lane & 3) == 0 && ((pub >> (lane >> 2)) & 1)) ? WS_PART + blockIdx.x * AREA + B * 4096 + (lane + w) * 16 : WS_BYTES;
    int o_d = WS_PART + blockIdx.x * AREA + w * 1024 + lane * 16;
    unsigned tl0 = __builtin_amdgcn_readlane(tl, 4 * b0), th0 = __builtin_amdgcn_readlane(th, 4 * b0);
    int i0_ = 4 * b0, i1_ = i0_ + 1, i2_ = i0_ + 2, i3_ = i0_ + 3, so0 = b0 * 4096;
    asm volatile("" : "+v"(a_rec), "+v"(a_mlw), "+v"(a_mlr), "+v"(a_rd), "+v"(o_ml), "+v"(o_d), "+v"(a_dn), "+v"(one), "+v"(a_cnt), "+v"(a_rd0));
    asm volatile("" : "+s"(tl0), "+s"(th0), "+s"(i0_), "+s"(i1_), "+s"(i2_), "+s"(i3_), "+s"(so0));

    f32x4 acc[2][8];
    float m[2], l[2];
#pragma unroll
    for (int x = 0; x < 2; ++x) { m[x] = -FLT_MAX; l[x] = 0.f;
#pragma unroll
      for (int d = 0; d < 8; ++d) acc[x][d] = f32x4{0.f, 0.f, 0.f, 0.f}; }

    asm volatile("" : "+v"(qf[0][0]), "+v"(qf[0][1]), "+v"(qf[0][2]), "+v"(qf[0][3]), "+v"(qf[1][0]), "+v"(qf[1][1]), "+v"(qf[1][2]), "+v"(qf[1][3]));
    for (int i0 = 0; i0 < n; i0 += NS) {
#pragma unroll
      for (int u = 0; u < NS; ++u) {
        const int i = i0 + u;
        if (u && i >= n) break;
        const int jb = __builtin_amdgcn_readlane(djb, i);
        const unsigned so = __builtin_amdgcn_readlane(dphys, i + NS);
        const int rem = n - i;
        if (rem == 1) asm volatile("s_waitcnt vmcnt(4)" ::: "memory");
        if (NS == 3 && rem == 2) asm volatile("s_waitcnt vmcnt(12)" ::: "memory");
        asm volatile("s_waitcnt vmcnt(%0)" :: "i"(NS * 8 - 4) : "memory");
        if (i == 0) STAMP(3);
        u32x4 kf[4];
#pragma unroll
        for (int kk = 0; kk < 4; ++kk) kf[kk] = lds_ld<u32x4>(lds, ka[kk] + u * 8192);
        asm volatile("s_waitcnt lgkmcnt(0)" : "+v"(kf[0]), "+v"(kf[1]), "+v"(kf[2]), "+v"(kf[3]) :: "memory");
        if (rem > NS) dma4(kvo, rk, so, ring + u * 8192);
        u32x2 pb[2];
#pragma unroll
        for (int x = 0; x < 2; ++x) {
          if (jb & (0xF0000 << (4 * x))) {
            f32x4 s = {0.f, 0.f, 0.f, 0.f};
#pragma unroll
            for (int kk = 0; kk < 4; ++kk)
              s = __builtin_amdgcn_mfma_f32_16x16x32_bf16(__builtin_bit_cast(bf16x8, kf[kk]), __builtin_bit_cast(bf16x8, qf[x][kk]), s, 0, 0, 0);
            const int lim = (jb & bbit[x]) ? limv[x] - (jb & 0xFFFF) : 0;
#pragma unroll
            for (int r = 0; r < 4; ++r) s[r] = (r < lim) ? s[r] : -INFINITY;
            float mx = fmaxf(fmaxf(s[0], s[1]), fmaxf(s[2], s[3]));
            mx = xg_max(mx) * CSC;
            const float mn = fmaxf(m[x], mx);
            const float al = __builtin_amdgcn_exp2f(m[x] - mn);
            m[x] = mn;
            float p[4];
#pragma unroll
            for (int r = 0; r < 4; ++r) p[r] = __builtin_amdgcn_exp2f(__builtin_fmaf(s[r], CSC, -mn));
            l[x] = __builtin_fmaf(l[x], al, (p[0] + p[1]) + (p[2] + p[3]));
            pb[x] = u32x2{cvtpk(p[0], p[1]), cvtpk(p[2], p[3])};
#pragma unroll
            for (int d = 0; d < 8; ++d) acc[x][d] *= al;
          }
        }
        if (rem == 1) asm volatile("s_waitcnt vmcnt(0)" ::: "memory");
        if (rem == 2) asm volatile("s_waitcnt vmcnt(8)" ::: "memory");
        if (NS == 3 && rem == 3) asm volatile("s_waitcnt vmcnt(16)" ::: "memory");
        asm volatile("s_waitcnt vmcnt(%0)" :: "i"(NS * 8 - 4) : "memory");
        s16x4 vt[8];
#pragma unroll
        for (int d = 0; d < 8; ++d) {
          typedef __attribute__((ext_vector_type(4))) short v4i16;
          vt[d] = __builtin_amdgcn_ds_read_tr16_b64_v4i16((__attribute__((address_space(3))) v4i16*)(lds + (va[d] + u * 8192)));
        }
        asm volatile("s_waitcnt lgkmcnt(0)" : "+v"(vt[0]), "+v"(vt[1]), "+v"(vt[2]), "+v"(vt[3]), "+v"(vt[4]), "+v"(vt[5]), "+v"(vt[6]), "+v"(vt[7]) :: "memory");
        if (rem > NS) dma4(vvo, rv, so, ring + u * 8192 + 4096);
#pragma unroll
        for (int x = 0; x < 2; ++x) {
          if (jb & (0xF0000 << (4 * x))) {
#pragma unroll
            for (int d = 0; d < 8; ++d)
              acc[x][d] = __builtin_amdgcn_mfma_f32_16x16x16bf16_1k(vt[d], __builtin_bit_cast(s16x4, pb[x]), acc[x][d], 0, 0, 0);
          }
        }
      }
    }
    STAMP(4);
#pragma unroll
    for (int x = 0; x < 2; ++x) {
      if ((wgmask | ownmask) & (0xF << (4 * x))) {
#pragma unroll
        for (int d = 0; d < 8; ++d) lds_vst<f32x4>(lds, a_rec + x * 16 * ROW + d * 64, acc[x][d]);
        lds_vst<f32x2>(lds, a_mlw + x * 512, f32x2{m[x], xg_sum(l[x])});
      }
    }
    lds_vst<unsigned char>(lds, a_dn, (unsigned char)one);
    if (pub) {
      f32x2 ml, xv[NSTR];
#pragma unroll 1
      for (int spin = 0; spin < (1 << 20); ++spin) {
        int done;
        rec_reads(done, ml, xv, a_cnt, a_mlr, a_rd0);
        const int dn = __builtin_amdgcn_readfirstlane(done);
        if (dn == ALL_DONE) break;
        if (__builtin_popcount(dn) < NSTR - 1) __builtin_amdgcn_s_sleep(2);
      }
      if (wgmask == 0xFF) {
        f32x2 xa[B][NSTR];
#pragma unroll
        for (int b = 0; b < B; ++b)
#pragma unroll
          for (int k = 0; k < NSTR; ++k) xa[b][k] = lds_vld<f32x2>(lds, a_rd + b * 4 * ROW + k * RECS);
        const float M = quad_max(ml[0]);
        const float wt = __builtin_amdgcn_exp2f(ml[0] - M);
        __builtin_amdgcn_raw_buffer_store_b128(seal(f32x2{M, quad_sum(wt * ml[1])}, tl, th), rw, o_ml, 0, 16);
#pragma unroll
        for (int b = 0; b < B; ++b) {
          f32x2 o = rdl(wt, 4 * b) * xa[b][0];
#pragma unroll
          for (int k = 1; k < NSTR; ++k) o += rdl(wt, 4 * b + k) * xa[b][k];
          __builtin_amdgcn_raw_buffer_store_b128(seal(o, __builtin_amdgcn_readlane(tl, 4 * b), __builtin_amdgcn_readlane(th, 4 * b)), rw, o_d, b * 4096, 16);
        }
      } else {
        const float M = quad_max(ml[0]);
        const float wt = __builtin_amdgcn_exp2f(ml[0] - M);
        const f32x2 o0 = rdl(wt, i0_) * xv[0] + rdl(wt, i1_) * xv[1] + rdl(wt, i2_) * xv[2] + rdl(wt, i3_) * xv[3];
        __builtin_amdgcn_raw_buffer_store_b128(seal(o0, tl0, th0), rw, o_d, so0, 16);
        __builtin_amdgcn_raw_buffer_store_b128(seal(f32x2{M, quad_sum(wt * ml[1])}, tl, th), rw, o_ml, 0, 16);
        for (int pm = pub & (pub - 1); pm; pm &= pm - 1) {
          const int b = __builtin_ctz(pm);
          f32x2 o = rdl(wt, 4 * b) * lds_vld<f32x2>(lds, a_rd + b * 4 * ROW);
#pragma unroll
          for (int k = 1; k < NSTR; ++k) o += rdl(wt, 4 * b + k) * lds_vld<f32x2>(lds, a_rd + b * 4 * ROW + k * RECS);
          __builtin_amdgcn_raw_buffer_store_b128(seal(o, __builtin_amdgcn_readlane(tl, 4 * b), __builtin_amdgcn_readlane(th, 4 * b)), rw, o_d, b * 4096, 16);
        }
      }
    }
    STAMP(5);
  } else {
    const int r = w - NSTR, k16 = lane & 15;
    if (r == 0) { unsigned dummy; asm volatile("buffer_load_dword %0, off, %1, %2 sc1\n\ts_waitcnt vmcnt(0)" : "=v"(dummy) : "s"(rw), "s"(WS_PART + blockIdx.x * AREA + AREA - 128) : "memory"); }
    if (ownmask) {
      const int yj = lane * wr, yn = lane + 1 >= nw ? 0x7FFFFFFF : yj + wr;
      int cj = (yj - min((yj >> 8) * SHR_A, ash)) >> 16, cn = (yn - min((yn >> 8) * SHR_A, ash)) >> 16;
#pragma unroll
      for (int b = 0; b < B; ++b) { const int ym = __builtin_amdgcn_readlane(ymid, b); cj += ym < yj ? beta : 0; cn += ym < yn ? beta : 0; }
      cj = min(cj, Wp); cn = min(cn, Wp);
      const int hb = WS_PART + (blockIdx.x - wi) * AREA;
      const int vo = r * 1024 + lane * 16;
      int spin = 0;
      for (int om = ownmask; om; om &= om - 1) {
        const int b = __builtin_ctz(om), row = b * 4 + r;
        const unsigned tlb = __builtin_amdgcn_readlane(tl, 4 * b), thb = __builtin_amdgcn_readlane(th, 4 * b);
        const bool con = (cj < S && cj < cn) || max(cj, __builtin_amdgcn_readlane(sb, b)) < min(cn, __builtin_amdgcn_readlane(se, b));
        unsigned long long pend = __ballot(con && lane < nw && lane != wi);
        float M = -FLT_MAX, L = 0.f; f32x2 o = {0.f, 0.f};
        const int lb = (int)(uintptr_t)lds;
        const int a_cnt = lb + L_CNT, a_ml = lb + L_ML + row * 32 + (lane & 3) * 8, a_x = lb + L_REC + row * ROW + lane * 8;
        int left = __builtin_popcountll(pend);
        do {
          const int nr = left > 2 * GK ? min(left - 2 * GK, 2 * GK) : left;
          left -= nr;
          const int nb[2] = {max(nr - GK, 0), min(nr, GK)};
          int so[2][GK], vml = WS_BYTES;
#pragma unroll
          for (int j = 0; j < 2; ++j)
#pragma unroll
            for (int k = 0; k < GK; ++k) {
              so[j][k] = hb + __builtin_ctzll(pend | (1ull << 63)) * AREA + b * 4096;
              vml = (k16 == j * GK + k && k < nb[j]) ? so[j][k] + (B - b) * 4096 + row * 16 : vml;
              pend = k < nb[j] ? pend & (pend - 1) : pend;
            }
          const bool ext = vml != WS_BYTES, mine = !left && k16 >= 2 * GK;
          u32x4 gm, gx[2][GK];
#pragma unroll
          for (int j = 0; j < 2; ++j)
#pragma unroll
            for (int k = 0; k < GK; ++k) gx[j][k] = u32x4{0, 0, 0, 0};
          int todo = 7, want = 4;
          f32x2 ml, xv[NSTR];
#pragma unroll 1
          for (; spin < (1 << 18); ++spin) {
            if (want & 4) gm = __builtin_amdgcn_raw_buffer_load_b128(rw, vml, 0, 16);
#pragma unroll
            for (int j = 0; j < 2; ++j)
              if (want & (1 << j)) {
#pragma unroll
                for (int k = 0; k < GK; ++k) { if (k >= nb[j]) break; gx[j][k] = __builtin_amdgcn_raw_buffer_load_b128(rw, vo, so[j][k], 16); }
              }
            int done;
            rec_reads(done, ml, xv, a_cnt, a_ml, a_x);
            const int dn = __builtin_amdgcn_readfirstlane(done);
            unsigned late = 0;
            if (want & 4) { late = (unsigned)__ballot(ext && stale(gm, tlb, thb) != 0); todo &= late ? 7 : 3; }
#pragma unroll
            for (int j = 0; j < 2; ++j)
              if (want & (1 << j)) {
                unsigned bad = 0;
#pragma unroll
                for (int k = 0; k < GK; ++k) { if (k >= nb[j]) break; bad |= stale(gx[j][k], tlb, thb); }
                todo &= __ballot(bad != 0) ? 7 : ~(1 << j);
              }
            if (!todo && (left || dn == ALL_DONE)) break;
            want = todo;
            if (__builtin_popcount(dn) < NSTR - 1) {
              want &= (late & ((1 << GK) - 1)) ? 6 : 7;
              want &= (late & (((1 << GK) - 1) << GK)) ? 5 : 7;
              if (want == 4) __builtin_amdgcn_s_sleep(LAZY);
              if (!want) __builtin_amdgcn_s_sleep(2);
            }
          }
          float mk = mine ? ml[0] : -FLT_MAX, lk = mine ? ml[1] : 0.f;
          mk = ext ? __uint_as_float(gm[0]) : mk; lk = ext ? __uint_as_float(gm[1]) : lk;
          const float Mn = row_max(fmaxf(mk, M));
          const float wt = __builtin_amdgcn_exp2f(mk - Mn), wc = __builtin_amdgcn_exp2f(M - Mn);
          M = Mn;
          L = __builtin_fmaf(L, wc, row_sum(wt * lk));
          o *= wc;
#pragma unroll
          for (int j = 0; j < 2; ++j)
#pragma unroll
            for (int k = 0; k < GK; ++k) o += rdl(wt, j * GK + k) * f32x2{__uint_as_float(gx[j][k][0]), __uint_as_float(gx[j][k][1])};
          if (!left) {
#pragma unroll
            for (int k = 0; k < NSTR; ++k) o += rdl(wt, 2 * GK + k) * xv[k];
          }
        } while (left);
        const int cx = __builtin_amdgcn_readlane(cxv, b);
        constexpr float MASKED = -998244352.0f * 1.4426950408889634f;
        if (cx < LK && MASKED - M > -200.f) {
          f32x2 sv = {0.f, 0.f};
          for (int t = cx; t < LK; ++t) {
            int ph = bt[b * NB + (t >> 4)];
            ph = ph < 0 ? ph + (int)nphys : ph;
            const unsigned vv = *(const __attribute__((address_space(1))) unsigned*)(vc + (size_t)ph * PAGE_BYTES + (t & 15) * 2048 + kvh * 256 + lane * 4);
            sv += f32x2{__uint_as_float(vv << 16), __uint_as_float(vv & 0xFFFF0000u)};
          }
          const float Mn = fmaxf(M, MASKED);
          const float a = __builtin_amdgcn_exp2f(M - Mn), wt = __builtin_amdgcn_exp2f(MASKED - Mn);
          L = L * a + wt * (float)(LK - cx);
          o = o * a + wt * sv;
        }
        const float inv = __builtin_amdgcn_rcpf(L);
        *(__attribute__((address_space(1))) unsigned*)(out + (b * H + kvh * G + r) * D + lane * 2) = cvtpk(o[0] * inv, o[1] * inv);
        if (tid == NSTR * 64) __hip_atomic_store((gu64p)(ws + WS_EPOCH) + kvh * B + b, ((unsigned long long)thb << 32) | tlb, __ATOMIC_RELAXED, __HIP_MEMORY_SCOPE_AGENT);
      }
    }
  }
  STAMP(6);
  STAMP_OUT();
}
}

extern "C" void kernel_launch(void* const* d_in, const int* in_sizes, int n_in, void* d_out, int out_size, void* d_ws, size_t ws_size,
                              hipStream_t stream) {
  attn_decode<<<256, NTHR, 0, stream>>>((const unsigned char*)d_in[0], (const unsigned char*)d_in[1], (const unsigned char*)d_in[2],
                                              (const int*)d_in[3], (const int*)d_in[4], (unsigned char*)d_ws, (unsigned short*)d_out,
                                              (long long)(in_sizes[1] / (PAGE * HKV * D)));
}
```

```cpp
#define PROBE 0
#include <hip/hip_runtime.h>
#include <hip/hip_bf16.h>
#include <stdint.h>
#include <float.h>

#define NSLOW 38
#define NS 2
#define SHR_A 8
#define OWN_B 0
#define WARM 1
#define LAZY 16

namespace {
constexpr int B = 8, H = 32, HKV = 8, G = 4, D = 128, PAGE = 16, NB = 512, LK = NB * PAGE;
constexpr int NFAST = (128 - NSLOW) / 3;
static_assert(NSLOW + 3 * NFAST == 128 && NSLOW < 64, "");
constexpr int NWAVE = 8, NTHR = NWAVE * 64, NSTR = 4;
constexpr int PAGE_BYTES = PAGE * HKV * D * 2;
constexpr float CSC = 0.088388347648318440f * 1.4426950408889634f;
constexpr int GK = 6;
constexpr int ROW = 528, RECW = 32 * ROW;
constexpr int L_TBL = 0;
constexpr int L_Q = L_TBL + B * NB * 4;
constexpr int L_ML = L_Q + 32 * 256;
constexpr int L_FM = L_ML + 32 * 32;
constexpr int L_CNT = L_FM + 32;
constexpr int L_RING = NS == 2 ? 32768 - 4096 : 32768;
constexpr int RINGW = NS * 8192;
constexpr int L_REC = NS == 2 ? L_RING + NSTR * RINGW : L_RING;
constexpr int RECS = NS == 2 ? RECW : RINGW;
constexpr int L_END = L_REC + NSTR * RECS;
static_assert(L_CNT + 4 <= L_RING && L_END <= 163840, "");
constexpr int WS_EPOCH = 0;
constexpr int AREA = B * 4096 + 1024;
constexpr int WS_PART = 4096;
constexpr int WS_BYTES = WS_PART + 256 * AREA;
constexpr unsigned long long TAG_STEP = 0x9E3779B97F4A7C15ull;

typedef __attribute__((ext_vector_type(4))) unsigned u32x4;
typedef __attribute__((ext_vector_type(2))) unsigned u32x2;
typedef __attribute__((ext_vector_type(4))) float f32x4;
typedef __attribute__((ext_vector_type(2))) float f32x2;
typedef __attribute__((ext_vector_type(8))) __bf16 bf16x8;
typedef __attribute__((ext_vector_type(4))) short s16x4;
typedef __attribute__((address_space(3))) unsigned char* ldsp;
typedef __attribute__((address_space(1))) unsigned long long* gu64p;

__device__ __forceinline__ unsigned cvtpk(float lo, float hi) {
  typedef __attribute__((ext_vector_type(2))) __bf16 bf16x2;
  bf16x2 r = {(__bf16)lo, (__bf16)hi};
  return __builtin_bit_cast(unsigned, r);
}
__device__ __forceinline__ float xg_max(float x) {
  auto s = __builtin_amdgcn_permlane16_swap(__float_as_uint(x), __float_as_uint(x), false, false);
  x = fmaxf(__uint_as_float(s[0]), __uint_as_float(s[1]));
  auto t = __builtin_amdgcn_permlane32_swap(__float_as_uint(x), __float_as_uint(x), false, false);
  return fmaxf(__uint_as_float(t[0]), __uint_as_float(t[1]));
}
__device__ __forceinline__ float xg_sum(float x) {
  auto s = __builtin_amdgcn_permlane16_swap(__float_as_uint(x), __float_as_uint(x), false, false);
  x = __uint_as_float(s[0]) + __uint_as_float(s[1]);
  auto t = __builtin_amdgcn_permlane32_swap(__float_as_uint(x), __float_as_uint(x), false, false);
  return __uint_as_float(t[0]) + __uint_as_float(t[1]);
}
template <int SH> __device__ __forceinline__ int row_shr(int x) { return __builtin_amdgcn_update_dpp(0, x, 0x110 + SH, 0xF, 0xF, true); }
template <int CTL> __device__ __forceinline__ float dpp(float x) { return __int_as_float(__builtin_amdgcn_update_dpp(0, __float_as_int(x), CTL, 0xF, 0xF, true)); }
#define DPP2(op, a, b) "s_nop 1\n\t" op " %0, %0, %0 " a " row_mask:0xf bank_mask:0xf\n\ts_nop 1\n\t" op " %0, %0, %0 " b " row_mask:0xf bank_mask:0xf\n\t"
__device__ __forceinline__ float quad_max(float x) { asm(DPP2("v_max_f32_dpp", "quad_perm:[1,0,3,2]", "quad_perm:[2,3,0,1]") : "+v"(x)); return x; }
__device__ __forceinline__ float quad_sum(float x) { asm(DPP2("v_add_f32_dpp", "quad_perm:[1,0,3,2]", "quad_perm:[2,3,0,1]") : "+v"(x)); return x; }
__device__ __forceinline__ float row_max(float x) { asm(DPP2("v_max_f32_dpp", "row_ror:8", "row_ror:4") DPP2("v_max_f32_dpp", "row_ror:2", "row_ror:1") : "+v"(x)); return x; }
__device__ __forceinline__ float row_sum(float x) { asm(DPP2("v_add_f32_dpp", "row_ror:8", "row_ror:4") DPP2("v_add_f32_dpp", "row_ror:2", "row_ror:1") : "+v"(x)); return x; }
__device__ __forceinline__ void rec_reads(int& done, f32x2& ml, f32x2 (&xv)[NSTR], int a_cnt, int a_ml, int a_x) {
  asm volatile("ds_read_b32 %0, %6\n\tds_read_b64 %1, %7\n\tds_read_b64 %2, %8\n\tds_read_b64 %3, %8 offset:%9\n\tds_read_b64 %4, %8 offset:%10\n\tds_read_b64 %5, %8 offset:%11\n\ts_waitcnt lgkmcnt(0)"
               : "=&v"(done), "=&v"(ml), "=&v"(xv[0]), "=&v"(xv[1]), "=&v"(xv[2]), "=&v"(xv[3]) : "v"(a_cnt), "v"(a_ml), "v"(a_x), "i"(RECS), "i"(2 * RECS), "i"(3 * RECS) : "memory");
}
constexpr int ALL_DONE = 0x01010101;
__device__ __forceinline__ float rdl(float x, int l) { return __int_as_float(__builtin_amdgcn_readlane(__float_as_int(x), l)); }
template <typename T> __device__ __forceinline__ T lds_ld(ldsp p, int off) { return *(__attribute__((address_space(3))) T*)(p + off); }
template <typename T> __device__ __forceinline__ T lds_vld(ldsp p, int off) { return *(volatile __attribute__((address_space(3))) T*)(p + off); }
template <typename T> __device__ __forceinline__ void lds_st(ldsp p, int off, T v) { *(__attribute__((address_space(3))) T*)(p + off) = v; }
template <typename T> __device__ __forceinline__ void lds_vst(ldsp p, int off, T v) { *(volatile __attribute__((address_space(3))) T*)(p + off) = v; }
__device__ __forceinline__ u32x4 seal(f32x2 v, unsigned tl, unsigned th) {
  return u32x4{__float_as_uint(v[0]), __float_as_uint(v[1]), __float_as_uint(v[0]) ^ tl, __float_as_uint(v[1]) ^ th};
}
__device__ __forceinline__ unsigned stale(u32x4 g, unsigned tl, unsigned th) { return (g[0] ^ g[2] ^ tl) | (g[1] ^ g[3] ^ th); }

#define STAMP(i) do { st_[i] = __builtin_amdgcn_s_memrealtime(); } while (0)
#define STAMP_OUT() do { if (w == 0 && blockIdx.x == 0) { const unsigned long long lim_ = st_[PROBE] + 4000; while (__builtin_amdgcn_s_memrealtime() < lim_) __builtin_amdgcn_s_sleep(1); } } while (0)
__device__ __forceinline__ void dma4(const int (&vo)[4], u32x4 rs, unsigned soff, unsigned ldsaddr) {
  asm volatile("s_mov_b32 m0, %6\n\ts_nop 4\n\t"
               "buffer_load_dwordx4 %0, %4, %5 offen nt lds\n\t"
               "buffer_load_dwordx4 %1, %4, %5 offen offset:1024 nt lds\n\t"
               "buffer_load_dwordx4 %2, %4, %5 offen offset:2048 nt lds\n\t"
               "buffer_load_dwordx4 %3, %4, %5 offen offset:3072 nt lds"
               :: "v"(vo[0]), "v"(vo[1]), "v"(vo[2]), "v"(vo[3]), "s"(rs), "s"(soff), "s"(ldsaddr) : "memory");
}

__global__ void __launch_bounds__(NTHR, 2)
attn_decode(const unsigned char* __restrict__ q, const unsigned char* kc, const unsigned char* vc,
            const int* __restrict__ bt, const int* __restrict__ ctxl, unsigned char* ws, unsigned short* out, long long nphys) {
  __shared__ __attribute__((aligned(16))) unsigned char smem[L_END];
  const ldsp lds = (ldsp)smem;
  unsigned long long st_[8] = {0, 0, 0, 0, 0, 0, 0, 0};
  STAMP(0);
  const int tid = threadIdx.x, lane = tid & 63;
  const int w = __builtin_amdgcn_readfirstlane(tid >> 6);
  const int g = lane >> 4, c = lane & 15;
  const int bx = blockIdx.x & 127, h4 = (bx >= NFAST) + (bx >= NFAST + NSLOW) + (bx >= 2 * NFAST + NSLOW);
  const int kvh = (blockIdx.x >> 7) * 4 + h4, nw = h4 == 1 ? NSLOW : NFAST;
  int wi = bx - h4 * NFAST - (h4 >= 2 ? NSLOW - NFAST : 0), rnw = h4 == 1 ? (65536 + NSLOW - 1) / NSLOW : (65536 + NFAST - 1) / NFAST;
  asm volatile("" : "+s"(kc), "+s"(vc), "+s"(out), "+s"(nphys), "+s"(wi), "+s"(rnw));
  if (w < NSTR) __builtin_amdgcn_s_setprio(3);
  int cxv = ctxl[lane & 7];
  int limv[2] = {ctxl[c >> 2], ctxl[4 + (c >> 2)]};
  int e[B];
#pragma unroll
  for (int b = 0; b < B; ++b) e[b] = bt[b * NB + tid];
  const u32x4 qv = *reinterpret_cast<const u32x4*>(q + (w * H + kvh * G) * D * 2 + lane * 16);
  unsigned long long tag = __hip_atomic_load((gu64p)(ws + WS_EPOCH) + kvh * B + ((lane >> 2) & 7), __ATOMIC_RELAXED, __HIP_MEMORY_SCOPE_AGENT);
  int kvo[4], vvo[4];
#pragma unroll
  for (int t = 0; t < 4; ++t) {
    const int row = 4 * t + g;
    kvo[t] = row * 2048 + ((c ^ row) * 16) + kvh * 256 - t * 1024;
    vvo[t] = row * 2048 + ((c ^ ((row & 7) << 1)) * 16) + kvh * 256 - t * 1024;
  }
  int fw = w + NSTR * lane;
  asm volatile("" : "+v"(kvo[0]), "+v"(kvo[1]), "+v"(kvo[2]), "+v"(kvo[3]), "+v"(vvo[0]), "+v"(vvo[1]), "+v"(vvo[2]), "+v"(vvo[3]), "+v"(fw));
  cxv = min(max(cxv, 0), LK);
#pragma unroll
  for (int x = 0; x < 2; ++x) limv[x] = min(max(limv[x], 0), LK) - 4 * g;
  asm volatile("" : "+v"(limv[0]), "+v"(limv[1]));
  int npv = (cxv + PAGE - 1) / PAGE;
  int npmax;
  {
    int t = max(npv, row_shr<1>(npv)); t = max(t, row_shr<2>(t)); t = max(t, row_shr<4>(t));
    npmax = __builtin_amdgcn_readlane(t, 7);
  }
  bool mis = false;
#pragma unroll
  for (int b = 0; b < B; ++b) { lds_st<int>(lds, L_TBL + (b * NB + tid) * 4, e[b]); mis |= (e[b] != e[0]); }
  {
    const unsigned long long bal = __ballot(mis);
    int first = min(bal ? (w * 64 + __builtin_ctzll(bal)) : NB, npmax);
    first = npmax ? first : 1;
    if (lane < 2) lds_st<int>(lds, lane ? L_CNT : L_FM + w * 4, lane ? 0 : first);
  }
  lds_st<u32x4>(lds, L_Q + (4 * w + g) * 256 + ((c ^ ((4 * w + g) & 15)) * 16), qv);
  tag += TAG_STEP;
  unsigned tl = (unsigned)tag, th = (unsigned)(tag >> 32);
  asm volatile("" : "+v"(tl), "+v"(th));
  __syncthreads();
  STAMP(1);
  int S;
  {
    const u32x4 f0 = lds_ld<u32x4>(lds, L_FM), f1 = lds_ld<u32x4>(lds, L_FM + 16);
    const unsigned s = min(min(min(f0[0], f0[1]), min(f0[2], f0[3])), min(min(f1[0], f1[1]), min(f1[2], f1[3])));
    S = __builtin_amdgcn_readfirstlane((int)s);
  }
  const int cnt = max(npv - S, 0);
  int se = cnt + row_shr<1>(cnt); se += row_shr<2>(se); se += row_shr<4>(se); se += S;
  const int sb = se - cnt;
  const int Wp = __builtin_amdgcn_readlane(se, 7);
  const int beta = (Wp * OWN_B) >> 12, ash = S * (SHR_A << 8);
  const int wr = ((((Wp - B * beta) << 6) + (ash >> 10)) * rnw) >> 6;
  const int mid = cnt ? sb + (cnt >> 1) : ((2 * (lane & 7) + 1) * Wp) >> 4;
  const int ymid = max(((mid - (lane & 7) * beta) << 16) + ash, 0);
  const int y0 = wi * wr, y1 = wi + 1 == nw ? 0x7FFFFFFF : y0 + wr;
  const int m0 = (int)(__ballot(ymid < y0) & 0xFF), m1 = (int)(__ballot(ymid < y1) & 0xFF);
  const int c0 = min(((y0 - min((y0 >> 8) * SHR_A, ash)) >> 16) + beta * __builtin_popcount(m0), Wp);
  const int c1 = min(((y1 - min((y1 >> 8) * SHR_A, ash)) >> 16) + beta * __builtin_popcount(m1), Wp);
  const int ownmask = m1 & ~m0;
  const int priv = (int)(__ballot(max(sb, c0) < min(se, c1)) & 0xFF);
  const int wgmask = (c0 < S && c0 < c1) ? 0xFF : priv;
  const auto rw = __builtin_amdgcn_make_buffer_rsrc(ws, 0, WS_BYTES, 0x00020000);
  const int n = (c1 - c0 - w + NSTR - 1) >> 2;
  if (w < NSTR) {
    unsigned dphys; int djb;
    {
      const int f = min(c0 + fw, Wp - 1);
      const int dl = S + (lane & 7) * NB - sb;
      int idx = f;
      for (int pm = priv; pm; pm &= pm - 1) {
        const int b = __builtin_ctz(pm);
        const int s0 = __builtin_amdgcn_readlane(sb, b), d0 = __builtin_amdgcn_readlane(dl, b);
        idx = f >= s0 ? f + d0 : idx;
      }
      int ph = lds_ld<int>(lds, L_TBL + (idx & (B * NB - 1)) * 4);
      ph = ph < 0 ? ph + (int)nphys : ph;
      dphys = (unsigned)ph * PAGE_BYTES;
      djb = ((idx & (NB - 1)) * PAGE) | (f < S ? 0xFF0000 : 0x10000 << ((idx >> 9) & 7));
    }
    const unsigned cbytes = (unsigned)nphys * PAGE_BYTES;
    const u32x4 rk = {(unsigned)(uintptr_t)kc, (unsigned)((uintptr_t)kc >> 32), cbytes, 0x00020000u};
    const u32x4 rv = {(unsigned)(uintptr_t)vc, (unsigned)((uintptr_t)vc >> 32), cbytes, 0x00020000u};
    const unsigned ring = (unsigned)(uintptr_t)lds + L_RING + w * RINGW;
#pragma unroll
    for (int u = 0; u < NS; ++u) {
      if (u >= n) break;
      const unsigned so = __builtin_amdgcn_readlane(dphys, u);
      dma4(kvo, rk, so, ring + u * 8192);
      dma4(vvo, rv, so, ring + u * 8192 + 4096);
    }
    STAMP(2);
    u32x4 qf[2][4];
#pragma unroll
    for (int x = 0; x < 2; ++x)
#pragma unroll
      for (int kk = 0; kk < 4; ++kk) qf[x][kk] = lds_ld<u32x4>(lds, L_Q + (x * 16 + c) * 256 + (((4 * kk + g) ^ c) * 16));
    int bbit[2];
#pragma unroll
    for (int x = 0; x < 2; ++x) bbit[x] = 1 << (16 + 4 * x + (c >> 2));
    int ka[4], va[8];
#pragma unroll
    for (int kk = 0; kk < 4; ++kk) ka[kk] = L_RING + w * RINGW + c * 256 + (((4 * kk + g) ^ c) * 16);
#pragma unroll
    for (int d = 0; d < 8; ++d) va[d] = L_RING + w * RINGW + 4096 + (4 * g + (c >> 2)) * 256 + ((((4 * g + (c >> 2)) & 7) ^ d) << 5) + ((c & 3) << 3);
    const int pub = wgmask & ~ownmask;
    const int lb = (int)(uintptr_t)lds;
    int b0 = __builtin_ctz(pub | 0x80);
    int a_rec = L_REC + w * RECS + c * ROW + g * 16, a_mlw = L_ML + c * 32 + w * 8, a_dn = L_CNT + w, one = 1;
    int a_cnt = lb + L_CNT, a_mlr = lb + L_ML + (((lane >> 2) & 7) * 4 + w) * 32 + (lane & 3) * 8, a_rd = L_REC + w * ROW + lane * 8, a_rd0 = lb + a_rd + b0 * 4 * ROW;
    int o_ml = (lane < 32 && (lane & 3) == 0 && ((pub >> (lane >> 2)) & 1)) ? WS_PART + blockIdx.x * AREA + B * 4096 + (lane + w) * 16 : WS_BYTES;
    int o_d = WS_PART + blockIdx.x * AREA + w * 1024 + lane * 16;
    unsigned tl0 = __builtin_amdgcn_readlane(tl, 4 * b0), th0 = __builtin_amdgcn_readlane(th, 4 * b0);
    int i0_ = 4 * b0, i1_ = i0_ + 1, i2_ = i0_ + 2, i3_ = i0_ + 3, so0 = b0 * 4096;
    asm volatile("" : "+v"(a_rec), "+v"(a_mlw), "+v"(a_mlr), "+v"(a_rd), "+v"(o_ml), "+v"(o_d), "+v"(a_dn), "+v"(one), "+v"(a_cnt), "+v"(a_rd0));
    asm volatile("" : "+s"(tl0), "+s"(th0), "+s"(i0_), "+s"(i1_), "+s"(i2_), "+s"(i3_), "+s"(so0));

    f32x4 acc[2][8];
    float m[2], l[2];
#pragma unroll
    for (int x = 0; x < 2; ++x) { m[x] = -FLT_MAX; l[x] = 0.f;
#pragma unroll
      for (int d = 0; d < 8; ++d) acc[x][d] = f32x4{0.f, 0.f, 0.f, 0.f}; }

    asm volatile("" : "+v"(qf[0][0]), "+v"(qf[0][1]), "+v"(qf[0][2]), "+v"(qf[0][3]), "+v"(qf[1][0]), "+v"(qf[1][1]), "+v"(qf[1][2]), "+v"(qf[1][3]));
    for (int i0 = 0; i0 < n; i0 += NS) {
#pragma unroll
      for (int u = 0; u < NS; ++u) {
        const int i = i0 + u;
        if (u && i >= n) break;
        const int jb = __builtin_amdgcn_readlane(djb, i);
        const unsigned so = __builtin_amdgcn_readlane(dphys, i + NS);
        const int rem = n - i;
        if (rem == 1) asm volatile("s_waitcnt vmcnt(4)" ::: "memory");
        if (NS == 3 && rem == 2) asm volatile("s_waitcnt vmcnt(12)" ::: "memory");
        asm volatile("s_waitcnt vmcnt(%0)" :: "i"(NS * 8 - 4) : "memory");
        if (i == 0) STAMP(3);
        u32x4 kf[4];
#pragma unroll
        for (int kk = 0; kk < 4; ++kk) kf[kk] = lds_ld<u32x4>(lds, ka[kk] + u * 8192);
        asm volatile("s_waitcnt lgkmcnt(0)" : "+v"(kf[0]), "+v"(kf[1]), "+v"(kf[2]), "+v"(kf[3]) :: "memory");
        if (rem > NS) dma4(kvo, rk, so, ring + u * 8192);
        u32x2 pb[2];
#pragma unroll
        for (int x = 0; x < 2; ++x) {
          if (jb & (0xF0000 << (4 * x))) {
            f32x4 s = {0.f, 0.f, 0.f, 0.f};
#pragma unroll
            for (int kk = 0; kk < 4; ++kk)
              s = __builtin_amdgcn_mfma_f32_16x16x32_bf16(__builtin_bit_cast(bf16x8, kf[kk]), __builtin_bit_cast(bf16x8, qf[x][kk]), s, 0, 0, 0);
            const int lim = (jb & bbit[x]) ? limv[x] - (jb & 0xFFFF) : 0;
#pragma unroll
            for (int r = 0; r < 4; ++r) s[r] = (r < lim) ? s[r] : -INFINITY;
            float mx = fmaxf(fmaxf(s[0], s[1]), fmaxf(s[2], s[3]));
            mx = xg_max(mx) * CSC;
            const float mn = fmaxf(m[x], mx);
            const float al = __builtin_amdgcn_exp2f(m[x] - mn);
            m[x] = mn;
            float p[4];
#pragma unroll
            for (int r = 0; r < 4; ++r) p[r] = __builtin_amdgcn_exp2f(__builtin_fmaf(s[r], CSC, -mn));
            l[x] = __builtin_fmaf(l[x], al, (p[0] + p[1]) + (p[2] + p[3]));
            pb[x] = u32x2{cvtpk(p[0], p[1]), cvtpk(p[2], p[3])};
#pragma unroll
            for (int d = 0; d < 8; ++d) acc[x][d] *= al;
          }
        }
        if (rem == 1) asm volatile("s_waitcnt vmcnt(0)" ::: "memory");
        if (rem == 2) asm volatile("s_waitcnt vmcnt(8)" ::: "memory");
        if (NS == 3 && rem == 3) asm volatile("s_waitcnt vmcnt(16)" ::: "memory");
        asm volatile("s_waitcnt vmcnt(%0)" :: "i"(NS * 8 - 4) : "memory");
        s16x4 vt[8];
#pragma unroll
        for (int d = 0; d < 8; ++d) {
          typedef __attribute__((ext_vector_type(4))) short v4i16;
          vt[d] = __builtin_amdgcn_ds_read_tr16_b64_v4i16((__attribute__((address_space(3))) v4i16*)(lds + (va[d] + u * 8192)));
        }
        asm volatile("s_waitcnt lgkmcnt(0)" : "+v"(vt[0]), "+v"(vt[1]), "+v"(vt[2]), "+v"(vt[3]), "+v"(vt[4]), "+v"(vt[5]), "+v"(vt[6]), "+v"(vt[7]) :: "memory");
        if (rem > NS) dma4(vvo, rv, so, ring + u * 8192 + 4096);
#pragma unroll
        for (int x = 0; x < 2; ++x) {
          if (jb & (0xF0000 << (4 * x))) {
#pragma unroll
            for (int d = 0; d < 8; ++d)
              acc[x][d] = __builtin_amdgcn_mfma_f32_16x16x16bf16_1k(vt[d], __builtin_bit_cast(s16x4, pb[x]), acc[x][d], 0, 0, 0);
          }
        }
      }
    }
    STAMP(4);
#pragma unroll
    for (int x = 0; x < 2; ++x) {
      if ((wgmask | ownmask) & (0xF << (4 * x))) {
#pragma unroll
        for (int d = 0; d < 8; ++d) lds_vst<f32x4>(lds, a_rec + x * 16 * ROW + d * 64, acc[x][d]);
        lds_vst<f32x2>(lds, a_mlw + x * 512, f32x2{m[x], xg_sum(l[x])});
      }
    }
    lds_vst<unsigned char>(lds, a_dn, (unsigned char)one);
    if (pub) {
      f32x2 ml, xv[NSTR];
#pragma unroll 1
      for (int spin = 0; spin < (1 << 20); ++spin) {
        int done;
        rec_reads(done, ml, xv, a_cnt, a_mlr, a_rd0);
        const int dn = __builtin_amdgcn_readfirstlane(done);
        if (dn == ALL_DONE) break;
        if (__builtin_popcount(dn) < NSTR - 1) __builtin_amdgcn_s_sleep(2);
      }
      if (wgmask == 0xFF) {
        f32x2 xa[B][NSTR];
#pragma unroll
        for (int b = 0; b < B; ++b)
#pragma unroll
          for (int k = 0; k < NSTR; ++k) xa[b][k] = lds_vld<f32x2>(lds, a_rd + b * 4 * ROW + k * RECS);
        const float M = quad_max(ml[0]);
        const float wt = __builtin_amdgcn_exp2f(ml[0] - M);
        __builtin_amdgcn_raw_buffer_store_b128(seal(f32x2{M, quad_sum(wt * ml[1])}, tl, th), rw, o_ml, 0, 16);
#pragma unroll
        for (int b = 0; b < B; ++b) {
          f32x2 o = rdl(wt, 4 * b) * xa[b][0];
#pragma unroll
          for (int k = 1; k < NSTR; ++k) o += rdl(wt, 4 * b + k) * xa[b][k];
          __builtin_amdgcn_raw_buffer_store_b128(seal(o, __builtin_amdgcn_readlane(tl, 4 * b), __builtin_amdgcn_readlane(th, 4 * b)), rw, o_d, b * 4096, 16);
        }
      } else {
        const float M = quad_max(ml[0]);
        const float wt = __builtin_amdgcn_exp2f(ml[0] - M);
        const f32x2 o0 = rdl(wt, i0_) * xv[0] + rdl(wt, i1_) * xv[1] + rdl(wt, i2_) * xv[2] + rdl(wt, i3_) * xv[3];
        __builtin_amdgcn_raw_buffer_store_b128(seal(o0, tl0, th0), rw, o_d, so0, 16);
        __builtin_amdgcn_raw_buffer_store_b128(seal(f32x2{M, quad_sum(wt * ml[1])}, tl, th), rw, o_ml, 0, 16);
        for (int pm = pub & (pub - 1); pm; pm &= pm - 1) {
          const int b = __builtin_ctz(pm);
          f32x2 o = rdl(wt, 4 * b) * lds_vld<f32x2>(lds, a_rd + b * 4 * ROW);
#pragma unroll
          for (int k = 1; k < NSTR; ++k) o += rdl(wt, 4 * b + k) * lds_vld<f32x2>(lds, a_rd + b * 4 * ROW + k * RECS);
          __builtin_amdgcn_raw_buffer_store_b128(seal(o, __builtin_amdgcn_readlane(tl, 4 * b), __builtin_amdgcn_readlane(th, 4 * b)), rw, o_d, b * 4096, 16);
        }
      }
    }
    STAMP(5);
  } else {
    const int r = w - NSTR, k16 = lane & 15;
    if (r == 0) { unsigned dummy; asm volatile("buffer_load_dword %0, off, %1, %2 sc1\n\ts_waitcnt vmcnt(0)" : "=v"(dummy) : "s"(rw), "s"(WS_PART + blockIdx.x * AREA + AREA - 128) : "memory"); }
    if (ownmask) {
      const int yj = lane * wr, yn = lane + 1 >= nw ? 0x7FFFFFFF : yj + wr;
      int cj = (yj - min((yj >> 8) * SHR_A, ash)) >> 16, cn = (yn - min((yn >> 8) * SHR_A, ash)) >> 16;
#pragma unroll
      for (int b = 0; b < B; ++b) { const int ym = __builtin_amdgcn_readlane(ymid, b); cj += ym < yj ? beta : 0; cn += ym < yn ? beta : 0; }
      cj = min(cj, Wp); cn = min(cn, Wp);
      const int hb = WS_PART + (blockIdx.x - wi) * AREA;
      const int vo = r * 1024 + lane * 16;
      int spin = 0;
      for (int om = ownmask; om; om &= om - 1) {
        const int b = __builtin_ctz(om), row = b * 4 + r;
        const unsigned tlb = __builtin_amdgcn_readlane(tl, 4 * b), thb = __builtin_amdgcn_readlane(th, 4 * b);
        const bool con = (cj < S && cj < cn) || max(cj, __builtin_amdgcn_readlane(sb, b)) < min(cn, __builtin_amdgcn_readlane(se, b));
        unsigned long long pend = __ballot(con && lane < nw && lane != wi);
        float M = -FLT_MAX, L = 0.f; f32x2 o = {0.f, 0.f};
        const int lb = (int)(uintptr_t)lds;
        const int a_cnt = lb + L_CNT, a_ml = lb + L_ML + row * 32 + (lane & 3) * 8, a_x = lb + L_REC + row * ROW + lane * 8;
        int left = __builtin_popcountll(pend);
        do {
          const int nr = left > 2 * GK ? min(left - 2 * GK, 2 * GK) : left;
          left -= nr;
          const int nb[2] = {max(nr - GK, 0), min(nr, GK)};
          int so[2][GK], vml = WS_BYTES;
#pragma unroll
          for (int j = 0; j < 2; ++j)
#pragma unroll
            for (int k = 0; k < GK; ++k) {
              so[j][k] = hb + __builtin_ctzll(pend | (1ull << 63)) * AREA + b * 4096;
              vml = (k16 == j * GK + k && k < nb[j]) ? so[j][k] + (B - b) * 4096 + row * 16 : vml;
              pend = k < nb[j] ? pend & (pend - 1) : pend;
            }
          const bool ext = vml != WS_BYTES, mine = !left && k16 >= 2 * GK;
          u32x4 gm, gx[2][GK];
#pragma unroll
          for (int j = 0; j < 2; ++j)
#pragma unroll
            for (int k = 0; k < GK; ++k) gx[j][k] = u32x4{0, 0, 0, 0};
          int todo = 7, want = 4;
          f32x2 ml, xv[NSTR];
#pragma unroll 1
          for (; spin < (1 << 18); ++spin) {
            if (want & 4) gm = __builtin_amdgcn_raw_buffer_load_b128(rw, vml, 0, 16);
#pragma unroll
            for (int j = 0; j < 2; ++j)
              if (want & (1 << j)) {
#pragma unroll
                for (int k = 0; k < GK; ++k) { if (k >= nb[j]) break; gx[j][k] = __builtin_amdgcn_raw_buffer_load_b128(rw, vo, so[j][k], 16); }
              }
            int done;
            rec_reads(done, ml, xv, a_cnt, a_ml, a_x);
            const int dn = __builtin_amdgcn_readfirstlane(done);
            unsigned late = 0;
            if (want & 4) { late = (unsigned)__ballot(ext && stale(gm, tlb, thb) != 0); todo &= late ? 7 : 3; }
#pragma unroll
            for (int j = 0; j < 2; ++j)
              if (want & (1 << j)) {
                unsigned bad = 0;
#pragma unroll
                for (int k = 0; k < GK; ++k) { if (k >= nb[j]) break; bad |= stale(gx[j][k], tlb, thb); }
                todo &= __ballot(bad != 0) ? 7 : ~(1 << j);
              }
            if (!todo && (left || dn == ALL_DONE)) break;
            want = todo;
            if (__builtin_popcount(dn) < NSTR - 1) {
              want &= (late & ((1 << GK) - 1)) ? 6 : 7;
              want &= (late & (((1 << GK) - 1) << GK)) ? 5 : 7;
              if (want == 4) __builtin_amdgcn_s_sleep(LAZY);
              if (!want) __builtin_amdgcn_s_sleep(2);
            }
          }
          float mk = mine ? ml[0] : -FLT_MAX, lk = mine ? ml[1] : 0.f;
          mk = ext ? __uint_as_float(gm[0]) : mk; lk = ext ? __uint_as_float(gm[1]) : lk;
          const float Mn = row_max(fmaxf(mk, M));
          const float wt = __builtin_amdgcn_exp2f(mk - Mn), wc = __builtin_amdgcn_exp2f(M - Mn);
          M = Mn;
          L = __builtin_fmaf(L, wc, row_sum(wt * lk));
          o *= wc;
#pragma unroll
          for (int j = 0; j < 2; ++j)
#pragma unroll
            for (int k = 0; k < GK; ++k) o += rdl(wt, j * GK + k) * f32x2{__uint_as_float(gx[j][k][0]), __uint_as_float(gx[j][k][1])};
          if (!left) {
#pragma unroll
            for (int k = 0; k < NSTR; ++k) o += rdl(wt, 2 * GK + k) * xv[k];
          }
        } while (left);
        const int cx = __builtin_amdgcn_readlane(cxv, b);
        constexpr float MASKED = -998244352.0f * 1.4426950408889634f;
        if (cx < LK && MASKED - M > -200.f) {
          f32x2 sv = {0.f, 0.f};
          for (int t = cx; t < LK; ++t) {
            int ph = bt[b * NB + (t >> 4)];
            ph = ph < 0 ? ph + (int)nphys : ph;
            const unsigned vv = *(const __attribute__((address_space(1))) unsigned*)(vc + (size_t)ph * PAGE_BYTES + (t & 15) * 2048 + kvh * 256 + lane * 4);
            sv += f32x2{__uint_as_float(vv << 16), __uint_as_float(vv & 0xFFFF0000u)};
          }
          const float Mn = fmaxf(M, MASKED);
          const float a = __builtin_amdgcn_exp2f(M - Mn), wt = __builtin_amdgcn_exp2f(MASKED - Mn);
          L = L * a + wt * (float)(LK - cx);
          o = o * a + wt * sv;
        }
        const float inv = __builtin_amdgcn_rcpf(L);
        *(__attribute__((address_space(1))) unsigned*)(out + (b * H + kvh * G + r) * D + lane * 2) = cvtpk(o[0] * inv, o[1] * inv);
        if (tid == NSTR * 64) __hip_atomic_store((gu64p)(ws + WS_EPOCH) + kvh * B + b, ((unsigned long long)thb << 32) | tlb, __ATOMIC_RELAXED, __HIP_MEMORY_SCOPE_AGENT);
      }
    }
  }
  STAMP(6);
  STAMP_OUT();
}
}

extern "C" void kernel_launch(void* const* d_in, const int* in_sizes, int n_in, void* d_out, int out_size, void* d_ws, size_t ws_size,
                              hipStream_t stream) {
  attn_decode<<<256, NTHR, 0, stream>>>((const unsigned char*)d_in[0], (const unsigned char*)d_in[1], (const unsigned char*)d_in[2],
                                              (const int*)d_in[3], (const int*)d_in[4], (unsigned char*)d_ws, (unsigned short*)d_out,
                                              (long long)(in_sizes[1] / (PAGE * HKV * D)));
}
```

```cpp
#include <hip/hip_runtime.h>
#include <hip/hip_bf16.h>
#include <stdint.h>
#include <float.h>

#define NSLOW 38
#define NS 2
#define SHR_A 8
#define OWN_B 0
#define COMPM 0xF0000
#define LAZY 16

namespace {
constexpr int B = 8, H = 32, HKV = 8, G = 4, D = 128, PAGE = 16, NB = 512, LK = NB * PAGE;
constexpr int NFAST = (128 - NSLOW) / 3;
static_assert(NSLOW + 3 * NFAST == 128 && NSLOW < 64, "");
constexpr int NWAVE = 8, NTHR = NWAVE * 64, NSTR = 4;
constexpr int PAGE_BYTES = PAGE * HKV * D * 2;
constexpr float CSC = 0.088388347648318440f * 1.4426950408889634f;
constexpr int GK = 6;
constexpr int ROW = 528, RECW = 32 * ROW;
constexpr int L_TBL = 0;
constexpr int L_Q = L_TBL + B * NB * 4;
constexpr int L_ML = L_Q + 32 * 256;
constexpr int L_FM = L_ML + 32 * 32;
constexpr int L_CNT = L_FM + 32;
constexpr int L_RING = NS == 2 ? 32768 - 4096 : 32768;
constexpr int RINGW = NS * 8192;
constexpr int L_REC = NS == 2 ? L_RING + NSTR * RINGW : L_RING;
constexpr int RECS = NS == 2 ? RECW : RINGW;
constexpr int L_END = L_REC + NSTR * RECS;
static_assert(L_CNT + 4 <= L_RING && L_END <= 163840, "");
constexpr int WS_EPOCH = 0;
constexpr int AREA = B * 4096 + 1024;
constexpr int WS_PART = 4096;
constexpr int WS_BYTES = WS_PART + 256 * AREA;
constexpr unsigned long long TAG_STEP = 0x9E3779B97F4A7C15ull;

typedef __attribute__((ext_vector_type(4))) unsigned u32x4;
typedef __attribute__((ext_vector_type(2))) unsigned u32x2;
typedef __attribute__((ext_vector_type(4))) float f32x4;
typedef __attribute__((ext_vector_type(2))) float f32x2;
typedef __attribute__((ext_vector_type(8))) __bf16 bf16x8;
typedef __attribute__((ext_vector_type(4))) short s16x4;
typedef __attribute__((address_space(3))) unsigned char* ldsp;
typedef __attribute__((address_space(1))) unsigned long long* gu64p;

__device__ __forceinline__ unsigned cvtpk(float lo, float hi) {
  typedef __attribute__((ext_vector_type(2))) __bf16 bf16x2;
  bf16x2 r = {(__bf16)lo, (__bf16)hi};
  return __builtin_bit_cast(unsigned, r);
}
__device__ __forceinline__ float xg_max(float x) {
  auto s = __builtin_amdgcn_permlane16_swap(__float_as_uint(x), __float_as_uint(x), false, false);
  x = fmaxf(__uint_as_float(s[0]), __uint_as_float(s[1]));
  auto t = __builtin_amdgcn_permlane32_swap(__float_as_uint(x), __float_as_uint(x), false, false);
  return fmaxf(__uint_as_float(t[0]), __uint_as_float(t[1]));
}
__device__ __forceinline__ float xg_sum(float x) {
  auto s = __builtin_amdgcn_permlane16_swap(__float_as_uint(x), __float_as_uint(x), false, false);
  x = __uint_as_float(s[0]) + __uint_as_float(s[1]);
  auto t = __builtin_amdgcn_permlane32_swap(__float_as_uint(x), __float_as_uint(x), false, false);
  return __uint_as_float(t[0]) + __uint_as_float(t[1]);
}
template <int SH> __device__ __forceinline__ int row_shr(int x) { return __builtin_amdgcn_update_dpp(0, x, 0x110 + SH, 0xF, 0xF, true); }
template <int CTL> __device__ __forceinline__ float dpp(float x) { return __int_as_float(__builtin_amdgcn_update_dpp(0, __float_as_int(x), CTL, 0xF, 0xF, true)); }
#define DPP2(op, a, b) "s_nop 1\n\t" op " %0, %0, %0 " a " row_mask:0xf bank_mask:0xf\n\ts_nop 1\n\t" op " %0, %0, %0 " b " row_mask:0xf bank_mask:0xf\n\t"
__device__ __forceinline__ float quad_max(float x) { asm(DPP2("v_max_f32_dpp", "quad_perm:[1,0,3,2]", "quad_perm:[2,3,0,1]") : "+v"(x)); return x; }
__device__ __forceinline__ float quad_sum(float x) { asm(DPP2("v_add_f32_dpp", "quad_perm:[1,0,3,2]", "quad_perm:[2,3,0,1]") : "+v"(x)); return x; }
__device__ __forceinline__ float row_max(float x) { asm(DPP2("v_max_f32_dpp", "row_ror:8", "row_ror:4") DPP2("v_max_f32_dpp", "row_ror:2", "row_ror:1") : "+v"(x)); return x; }
__device__ __forceinline__ float row_sum(float x) { asm(DPP2("v_add_f32_dpp", "row_ror:8", "row_ror:4") DPP2("v_add_f32_dpp", "row_ror:2", "row_ror:1") : "+v"(x)); return x; }
__device__ __forceinline__ void rec_reads(int& done, f32x2& ml, f32x2 (&xv)[NSTR], int a_cnt, int a_ml, int a_x) {
  asm volatile("ds_read_b32 %0, %6\n\tds_read_b64 %1, %7\n\tds_read_b64 %2, %8\n\tds_read_b64 %3, %8 offset:%9\n\tds_read_b64 %4, %8 offset:%10\n\tds_read_b64 %5, %8 offset:%11\n\ts_waitcnt lgkmcnt(0)"
               : "=&v"(done), "=&v"(ml), "=&v"(xv[0]), "=&v"(xv[1]), "=&v"(xv[2]), "=&v"(xv[3]) : "v"(a_cnt), "v"(a_ml), "v"(a_x), "i"(RECS), "i"(2 * RECS), "i"(3 * RECS) : "memory");
}
constexpr int ALL_DONE = 0x01010101;
__device__ __forceinline__ float rdl(float x, int l) { return __int_as_float(__builtin_amdgcn_readlane(__float_as_int(x), l)); }
template <typename T> __device__ __forceinline__ T lds_ld(ldsp p, int off) { return *(__attribute__((address_space(3))) T*)(p + off); }
template <typename T> __device__ __forceinline__ T lds_vld(ldsp p, int off) { return *(volatile __attribute__((address_space(3))) T*)(p + off); }
template <typename T> __device__ __forceinline__ void lds_st(ldsp p, int off, T v) { *(__attribute__((address_space(3))) T*)(p + off) = v; }
template <typename T> __device__ __forceinline__ void lds_vst(ldsp p, int off, T v) { *(volatile __attribute__((address_space(3))) T*)(p + off) = v; }
__device__ __forceinline__ u32x4 seal(f32x2 v, unsigned tl, unsigned th) {
  return u32x4{__float_as_uint(v[0]), __float_as_uint(v[1]), __float_as_uint(v[0]) ^ tl, __float_as_uint(v[1]) ^ th};
}
__device__ __forceinline__ unsigned stale(u32x4 g, unsigned tl, unsigned th) { return (g[0] ^ g[2] ^ tl) | (g[1] ^ g[3] ^ th); }

__device__ __forceinline__ void dma4(const int (&vo)[4], u32x4 rs, unsigned soff, unsigned ldsaddr) {
  asm volatile("s_mov_b32 m0, %6\n\ts_nop 4\n\t"
               "buffer_load_dwordx4 %0, %4, %5 offen nt lds\n\t"
               "buffer_load_dwordx4 %1, %4, %5 offen offset:1024 nt lds\n\t"
               "buffer_load_dwordx4 %2, %4, %5 offen offset:2048 nt lds\n\t"
               "buffer_load_dwordx4 %3, %4, %5 offen offset:3072 nt lds"
               :: "v"(vo[0]), "v"(vo[1]), "v"(vo[2]), "v"(vo[3]), "s"(rs), "s"(soff), "s"(ldsaddr) : "memory");
}

__global__ void __launch_bounds__(NTHR, 2)
attn_decode(const unsigned char* __restrict__ q, const unsigned char* kc, const unsigned char* vc,
            const int* __restrict__ bt, const int* __restrict__ ctxl, unsigned char* ws, unsigned short* out, long long nphys) {
  __shared__ __attribute__((aligned(16))) unsigned char smem[L_END];
  const ldsp lds = (ldsp)smem;
  const int tid = threadIdx.x, lane = tid & 63;
  const int w = __builtin_amdgcn_readfirstlane(tid >> 6);
  const int g = lane >> 4, c = lane & 15;
  const int bx = blockIdx.x & 127, h4 = (bx >= NFAST) + (bx >= NFAST + NSLOW) + (bx >= 2 * NFAST + NSLOW);
  const int kvh = (blockIdx.x >> 7) * 4 + h4, nw = h4 == 1 ? NSLOW : NFAST;
  int wi = bx - h4 * NFAST - (h4 >= 2 ? NSLOW - NFAST : 0), rnw = h4 == 1 ? (65536 + NSLOW - 1) / NSLOW : (65536 + NFAST - 1) / NFAST;
  asm volatile("" : "+s"(kc), "+s"(vc), "+s"(out), "+s"(nphys), "+s"(wi), "+s"(rnw));
  if (w < NSTR) __builtin_amdgcn_s_setprio(3);
  int cxv = ctxl[lane & 7];
  int limv[2] = {ctxl[c >> 2], ctxl[4 + (c >> 2)]};
  int e[B];
#pragma unroll
  for (int b = 0; b < B; ++b) e[b] = bt[b * NB + tid];
  const u32x4 qv = *reinterpret_cast<const u32x4*>(q + (w * H + kvh * G) * D * 2 + lane * 16);
  unsigned long long tag = __hip_atomic_load((gu64p)(ws + WS_EPOCH) + kvh * B + ((lane >> 2) & 7), __ATOMIC_RELAXED, __HIP_MEMORY_SCOPE_AGENT);
  int kvo[4], vvo[4];
#pragma unroll
  for (int t = 0; t < 4; ++t) {
    const int row = 4 * t + g;
    kvo[t] = row * 2048 + ((c ^ row) * 16) + kvh * 256 - t * 1024;
    vvo[t] = row * 2048 + ((c ^ ((row & 7) << 1)) * 16) + kvh * 256 - t * 1024;
  }
  int fw = w + NSTR * lane;
  asm volatile("" : "+v"(kvo[0]), "+v"(kvo[1]), "+v"(kvo[2]), "+v"(kvo[3]), "+v"(vvo[0]), "+v"(vvo[1]), "+v"(vvo[2]), "+v"(vvo[3]), "+v"(fw));
  cxv = min(max(cxv, 0), LK);
#pragma unroll
  for (int x = 0; x < 2; ++x) limv[x] = min(max(limv[x], 0), LK) - 4 * g;
  asm volatile("" : "+v"(limv[0]), "+v"(limv[1]));
  int npv = (cxv + PAGE - 1) / PAGE;
  int npmax;
  {
    int t = max(npv, row_shr<1>(npv)); t = max(t, row_shr<2>(t)); t = max(t, row_shr<4>(t));
    npmax = __builtin_amdgcn_readlane(t, 7);
  }
  bool mis = false;
#pragma unroll
  for (int b = 0; b < B; ++b) { lds_st<int>(lds, L_TBL + (b * NB + tid) * 4, e[b]); mis |= (e[b] != e[0]); }
  {
    const unsigned long long bal = __ballot(mis);
    int first = min(bal ? (w * 64 + __builtin_ctzll(bal)) : NB, npmax);
    first = npmax ? first : 1;
    if (lane < 2) lds_st<int>(lds, lane ? L_CNT : L_FM + w * 4, lane ? 0 : first);
  }
  lds_st<u32x4>(lds, L_Q + (4 * w + g) * 256 + ((c ^ ((4 * w + g) & 15)) * 16), qv);
  tag += TAG_STEP;
  unsigned tl = (unsigned)tag, th = (unsigned)(tag >> 32);
  asm volatile("" : "+v"(tl), "+v"(th));
  __syncthreads();
  int S;
  {
    const u32x4 f0 = lds_ld<u32x4>(lds, L_FM), f1 = lds_ld<u32x4>(lds, L_FM + 16);
    const unsigned s = min(min(min(f0[0], f0[1]), min(f0[2], f0[3])), min(min(f1[0], f1[1]), min(f1[2], f1[3])));
    S = __builtin_amdgcn_readfirstlane((int)s);
  }
  const int cnt = max(npv - S, 0);
  int se = cnt + row_shr<1>(cnt); se += row_shr<2>(se); se += row_shr<4>(se); se += S;
  const int sb = se - cnt;
  const int Wp = __builtin_amdgcn_readlane(se, 7);
  const int beta = (Wp * OWN_B) >> 12, ash = S * (SHR_A << 8);
  const int wr = ((((Wp - B * beta) << 6) + (ash >> 10)) * rnw) >> 6;
  const int mid = cnt ? sb + (cnt >> 1) : ((2 * (lane & 7) + 1) * Wp) >> 4;
  const int ymid = max(((mid - (lane & 7) * beta) << 16) + ash, 0);
  const int y0 = wi * wr, y1 = wi + 1 == nw ? 0x7FFFFFFF : y0 + wr;
  const int m0 = (int)(__ballot(ymid < y0) & 0xFF), m1 = (int)(__ballot(ymid < y1) & 0xFF);
  const int c0 = min(((y0 - min((y0 >> 8) * SHR_A, ash)) >> 16) + beta * __builtin_popcount(m0), Wp);
  const int c1 = min(((y1 - min((y1 >> 8) * SHR_A, ash)) >> 16) + beta * __builtin_popcount(m1), Wp);
  const int ownmask = m1 & ~m0;
  const int priv = (int)(__ballot(max(sb, c0) < min(se, c1)) & 0xFF);
  const int wgmask = (c0 < S && c0 < c1) ? 0xFF : priv;
  const auto rw = __builtin_amdgcn_make_buffer_rsrc(ws, 0, WS_BYTES, 0x00020000);
  const int n = (c1 - c0 - w + NSTR - 1) >> 2;
  if (w < NSTR) {
    unsigned dphys; int djb;
    {
      const int f = (wi & 4) ? max(c1 - 1 - fw, 0) : min(c0 + fw, Wp - 1);
      const int dl = S + (lane & 7) * NB - sb;
      int idx = f;
      for (int pm = priv; pm; pm &= pm - 1) {
        const int b = __builtin_ctz(pm);
        const int s0 = __builtin_amdgcn_readlane(sb, b), d0 = __builtin_amdgcn_readlane(dl, b);
        idx = f >= s0 ? f + d0 : idx;
      }
      int ph = lds_ld<int>(lds, L_TBL + (idx & (B * NB - 1)) * 4);
      ph = ph < 0 ? ph + (int)nphys : ph;
      dphys = (unsigned)ph * PAGE_BYTES;
      djb = ((idx & (NB - 1)) * PAGE) | (f < S ? 0xFF0000 : 0x10000 << ((idx >> 9) & 7));
    }
    const unsigned cbytes = (unsigned)nphys * PAGE_BYTES;
    const u32x4 rk = {(unsigned)(uintptr_t)kc, (unsigned)((uintptr_t)kc >> 32), cbytes, 0x00020000u};
    const u32x4 rv = {(unsigned)(uintptr_t)vc, (unsigned)((uintptr_t)vc >> 32), cbytes, 0x00020000u};
    const unsigned ring = (unsigned)(uintptr_t)lds + L_RING + w * RINGW;
#pragma unroll
    for (int u = 0; u < NS; ++u) {
      if (u >= n) break;
      const unsigned so = __builtin_amdgcn_readlane(dphys, u);
      dma4(kvo, rk, so, ring + u * 8192);
      dma4(vvo, rv, so, ring + u * 8192 + 4096);
    }
    u32x4 qf[2][4];
#pragma unroll
    for (int x = 0; x < 2; ++x)
#pragma unroll
      for (int kk = 0; kk < 4; ++kk) qf[x][kk] = lds_ld<u32x4>(lds, L_Q + (x * 16 + c) * 256 + (((4 * kk + g) ^ c) * 16));
    int bbit[2];
#pragma unroll
    for (int x = 0; x < 2; ++x) bbit[x] = 1 << (16 + 4 * x + (c >> 2));
    int ka[4], va[8];
#pragma unroll
    for (int kk = 0; kk < 4; ++kk) ka[kk] = L_RING + w * RINGW + c * 256 + (((4 * kk + g) ^ c) * 16);
#pragma unroll
    for (int d = 0; d < 8; ++d) va[d] = L_RING + w * RINGW + 4096 + (4 * g + (c >> 2)) * 256 + ((((4 * g + (c >> 2)) & 7) ^ d) << 5) + ((c & 3) << 3);
    const int pub = wgmask & ~ownmask;
    const int lb = (int)(uintptr_t)lds;
    int b0 = __builtin_ctz(pub | 0x80);
    int a_rec = L_REC + w * RECS + c * ROW + g * 16, a_mlw = L_ML + c * 32 + w * 8, a_dn = L_CNT + w, one = 1;
    int a_cnt = lb + L_CNT, a_mlr = lb + L_ML + (((lane >> 2) & 7) * 4 + w) * 32 + (lane & 3) * 8, a_rd = L_REC + w * ROW + lane * 8, a_rd0 = lb + a_rd + b0 * 4 * ROW;
    int o_ml = (lane < 32 && (lane & 3) == 0 && ((pub >> (lane >> 2)) & 1)) ? WS_PART + blockIdx.x * AREA + B * 4096 + (lane + w) * 16 : WS_BYTES;
    int o_d = WS_PART + blockIdx.x * AREA + w * 1024 + lane * 16;
    unsigned tl0 = __builtin_amdgcn_readlane(tl, 4 * b0), th0 = __builtin_amdgcn_readlane(th, 4 * b0);
    int i0_ = 4 * b0, i1_ = i0_ + 1, i2_ = i0_ + 2, i3_ = i0_ + 3, so0 = b0 * 4096;
    asm volatile("" : "+v"(a_rec), "+v"(a_mlw), "+v"(a_mlr), "+v"(a_rd), "+v"(o_ml), "+v"(o_d), "+v"(a_dn), "+v"(one), "+v"(a_cnt), "+v"(a_rd0));
    asm volatile("" : "+s"(tl0), "+s"(th0), "+s"(i0_), "+s"(i1_), "+s"(i2_), "+s"(i3_), "+s"(so0));

    f32x4 acc[2][8];
    float m[2], l[2];
#pragma unroll
    for (int x = 0; x < 2; ++x) { m[x] = -FLT_MAX; l[x] = 0.f;
#pragma unroll
      for (int d = 0; d < 8; ++d) acc[x][d] = f32x4{0.f, 0.f, 0.f, 0.f}; }

    asm volatile("" : "+v"(qf[0][0]), "+v"(qf[0][1]), "+v"(qf[0][2]), "+v"(qf[0][3]), "+v"(qf[1][0]), "+v"(qf[1][1]), "+v"(qf[1][2]), "+v"(qf[1][3]));
    for (int i0 = 0; i0 < n; i0 += NS) {
#pragma unroll
      for (int u = 0; u < NS; ++u) {
        const int i = i0 + u;
        if (u && i >= n) break;
        const int jb = __builtin_amdgcn_readlane(djb, i);
        const unsigned so = __builtin_amdgcn_readlane(dphys, i + NS);
        const int rem = n - i;
        if (rem == 1) asm volatile("s_waitcnt vmcnt(4)" ::: "memory");
        if (NS == 3 && rem == 2) asm volatile("s_waitcnt vmcnt(12)" ::: "memory");
        asm volatile("s_waitcnt vmcnt(%0)" :: "i"(NS * 8 - 4) : "memory");
        u32x4 kf[4];
#pragma unroll
        for (int kk = 0; kk < 4; ++kk) kf[kk] = lds_ld<u32x4>(lds, ka[kk] + u * 8192);
        asm volatile("s_waitcnt lgkmcnt(0)" : "+v"(kf[0]), "+v"(kf[1]), "+v"(kf[2]), "+v"(kf[3]) :: "memory");
        if (rem > NS) dma4(kvo, rk, so, ring + u * 8192);
        u32x2 pb[2];
#pragma unroll
        for (int x = 0; x < 2; ++x) {
          if (jb & (COMPM << (4 * x))) {
            f32x4 s = {0.f, 0.f, 0.f, 0.f};
#pragma unroll
            for (int kk = 0; kk < 4; ++kk)
              s = __builtin_amdgcn_mfma_f32_16x16x32_bf16(__builtin_bit_cast(bf16x8, kf[kk]), __builtin_bit_cast(bf16x8, qf[x][kk]), s, 0, 0, 0);
            const int lim = (jb & bbit[x]) ? limv[x] - (jb & 0xFFFF) : 0;
#pragma unroll
            for (int r = 0; r < 4; ++r) s[r] = (r < lim) ? s[r] : -INFINITY;
            float mx = fmaxf(fmaxf(s[0], s[1]), fmaxf(s[2], s[3]));
            mx = xg_max(mx) * CSC;
            const float mn = fmaxf(m[x], mx);
            const float al = __builtin_amdgcn_exp2f(m[x] - mn);
            m[x] = mn;
            float p[4];
#pragma unroll
            for (int r = 0; r < 4; ++r) p[r] = __builtin_amdgcn_exp2f(__builtin_fmaf(s[r], CSC, -mn));
            l[x] = __builtin_fmaf(l[x], al, (p[0] + p[1]) + (p[2] + p[3]));
            pb[x] = u32x2{cvtpk(p[0], p[1]), cvtpk(p[2], p[3])};
#pragma unroll
            for (int d = 0; d < 8; ++d) acc[x][d] *= al;
          }
        }
        if (rem == 1) asm volatile("s_waitcnt vmcnt(0)" ::: "memory");
        if (rem == 2) asm volatile("s_waitcnt vmcnt(8)" ::: "memory");
        if (NS == 3 && rem == 3) asm volatile("s_waitcnt vmcnt(16)" ::: "memory");
        asm volatile("s_waitcnt vmcnt(%0)" :: "i"(NS * 8 - 4) : "memory");
        s16x4 vt[8];
#pragma unroll
        for (int d = 0; d < 8; ++d) {
          typedef __attribute__((ext_vector_type(4))) short v4i16;
          vt[d] = __builtin_amdgcn_ds_read_tr16_b64_v4i16((__attribute__((address_space(3))) v4i16*)(lds + (va[d] + u * 8192)));
        }
        asm volatile("s_waitcnt lgkmcnt(0)" : "+v"(vt[0]), "+v"(vt[1]), "+v"(vt[2]), "+v"(vt[3]), "+v"(vt[4]), "+v"(vt[5]), "+v"(vt[6]), "+v"(vt[7]) :: "memory");
        if (rem > NS) dma4(vvo, rv, so, ring + u * 8192 + 4096);
#pragma unroll
        for (int x = 0; x < 2; ++x) {
          if (jb & (COMPM << (4 * x))) {
#pragma unroll
            for (int d = 0; d < 8; ++d)
              acc[x][d] = __builtin_amdgcn_mfma_f32_16x16x16bf16_1k(vt[d], __builtin_bit_cast(s16x4, pb[x]), acc[x][d], 0, 0, 0);
          }
        }
      }
    }
#pragma unroll
    for (int x = 0; x < 2; ++x) {
      if ((wgmask | ownmask) & (0xF << (4 * x))) {
#pragma unroll
        for (int d = 0; d < 8; ++d) lds_vst<f32x4>(lds, a_rec + x * 16 * ROW + d * 64, acc[x][d]);
        lds_vst<f32x2>(lds, a_mlw + x * 512, f32x2{m[x], xg_sum(l[x])});
      }
    }
    lds_vst<unsigned char>(lds, a_dn, (unsigned char)one);
    if (pub) {
      f32x2 ml, xv[NSTR];
#pragma unroll 1
      for (int spin = 0; spin < (1 << 20); ++spin) {
        int done;
        rec_reads(done, ml, xv, a_cnt, a_mlr, a_rd0);
        const int dn = __builtin_amdgcn_readfirstlane(done);
        if (dn == ALL_DONE) break;
        if (__builtin_popcount(dn) < NSTR - 1) __builtin_amdgcn_s_sleep(2);
      }
      if (wgmask == 0xFF) {
        f32x2 xa[B][NSTR];
#pragma unroll
        for (int b = 0; b < B; ++b)
#pragma unroll
          for (int k = 0; k < NSTR; ++k) xa[b][k] = lds_vld<f32x2>(lds, a_rd + b * 4 * ROW + k * RECS);
        const float M = quad_max(ml[0]);
        const float wt = __builtin_amdgcn_exp2f(ml[0] - M);
        __builtin_amdgcn_raw_buffer_store_b128(seal(f32x2{M, quad_sum(wt * ml[1])}, tl, th), rw, o_ml, 0, 16);
#pragma unroll
        for (int b = 0; b < B; ++b) {
          f32x2 o = rdl(wt, 4 * b) * xa[b][0];
#pragma unroll
          for (int k = 1; k < NSTR; ++k) o += rdl(wt, 4 * b + k) * xa[b][k];
          __builtin_amdgcn_raw_buffer_store_b128(seal(o, __builtin_amdgcn_readlane(tl, 4 * b), __builtin_amdgcn_readlane(th, 4 * b)), rw, o_d, b * 4096, 16);
        }
      } else {
        const float M = quad_max(ml[0]);
        const float wt = __builtin_amdgcn_exp2f(ml[0] - M);
        const f32x2 o0 = rdl(wt, i0_) * xv[0] + rdl(wt, i1_) * xv[1] + rdl(wt, i2_) * xv[2] + rdl(wt, i3_) * xv[3];
        __builtin_amdgcn_raw_buffer_store_b128(seal(o0, tl0, th0), rw, o_d, so0, 16);
        __builtin_amdgcn_raw_buffer_store_b128(seal(f32x2{M, quad_sum(wt * ml[1])}, tl, th), rw, o_ml, 0, 16);
        for (int pm = pub & (pub - 1); pm; pm &= pm - 1) {
          const int b = __builtin_ctz(pm);
          f32x2 o = rdl(wt, 4 * b) * lds_vld<f32x2>(lds, a_rd + b * 4 * ROW);
#pragma unroll
          for (int k = 1; k < NSTR; ++k) o += rdl(wt, 4 * b + k) * lds_vld<f32x2>(lds, a_rd + b * 4 * ROW + k * RECS);
          __builtin_amdgcn_raw_buffer_store_b128(seal(o, __builtin_amdgcn_readlane(tl, 4 * b), __builtin_amdgcn_readlane(th, 4 * b)), rw, o_d, b * 4096, 16);
        }
      }
    }
  } else {
    const int r = w - NSTR, k16 = lane & 15;
    if (r == 0) { unsigned dummy; asm volatile("buffer_load_dword %0, off, %1, %2 sc1\n\ts_waitcnt vmcnt(0)" : "=v"(dummy) : "s"(rw), "s"(WS_PART + blockIdx.x * AREA + AREA - 128) : "memory"); }
    if (ownmask) {
      const int yj = lane * wr, yn = lane + 1 >= nw ? 0x7FFFFFFF : yj + wr;
      int cj = (yj - min((yj >> 8) * SHR_A, ash)) >> 16, cn = (yn - min((yn >> 8) * SHR_A, ash)) >> 16;
#pragma unroll
      for (int b = 0; b < B; ++b) { const int ym = __builtin_amdgcn_readlane(ymid, b); cj += ym < yj ? beta : 0; cn += ym < yn ? beta : 0; }
      cj = min(cj, Wp); cn = min(cn, Wp);
      const int hb = WS_PART + (blockIdx.x - wi) * AREA;
      const int vo = r * 1024 + lane * 16;
      int spin = 0;
      while (__builtin_amdgcn_readfirstlane(lds_vld<int>(lds, L_CNT)) == 0) __builtin_amdgcn_s_sleep(8);
      for (int om = ownmask; om; om &= om - 1) {
        const int b = __builtin_ctz(om), row = b * 4 + r;
        const unsigned tlb = __builtin_amdgcn_readlane(tl, 4 * b), thb = __builtin_amdgcn_readlane(th, 4 * b);
        const bool con = (cj < S && cj < cn) || max(cj, __builtin_amdgcn_readlane(sb, b)) < min(cn, __builtin_amdgcn_readlane(se, b));
        unsigned long long pend = __ballot(con && lane < nw && lane != wi);
        float M = -FLT_MAX, L = 0.f; f32x2 o = {0.f, 0.f};
        const int lb = (int)(uintptr_t)lds;
        const int a_cnt = lb + L_CNT, a_ml = lb + L_ML + row * 32 + (lane & 3) * 8, a_x = lb + L_REC + row * ROW + lane * 8;
        int left = __builtin_popcountll(pend);
        do {
          const int nr = left > 2 * GK ? min(left - 2 * GK, 2 * GK) : left;
          left -= nr;
          const int nb[2] = {max(nr - GK, 0), min(nr, GK)};
          int so[2][GK], vml = WS_BYTES;
#pragma unroll
          for (int j = 0; j < 2; ++j)
#pragma unroll
            for (int k = 0; k < GK; ++k) {
              so[j][k] = hb + __builtin_ctzll(pend | (1ull << 63)) * AREA + b * 4096;
              vml = (k16 == j * GK + k && k < nb[j]) ? so[j][k] + (B - b) * 4096 + row * 16 : vml;
              pend = k < nb[j] ? pend & (pend - 1) : pend;
            }
          const bool ext = vml != WS_BYTES, mine = !left && k16 >= 2 * GK;
          u32x4 gm, gx[2][GK];
#pragma unroll
          for (int j = 0; j < 2; ++j)
#pragma unroll
            for (int k = 0; k < GK; ++k) gx[j][k] = u32x4{0, 0, 0, 0};
          int todo = 7, want = 4;
          f32x2 ml, xv[NSTR];
#pragma unroll 1
          for (; spin < (1 << 18); ++spin) {
            if (want & 4) gm = __builtin_amdgcn_raw_buffer_load_b128(rw, vml, 0, 16);
#pragma unroll
            for (int j = 0; j < 2; ++j)
              if (want & (1 << j)) {
#pragma unroll
                for (int k = 0; k < GK; ++k) { if (k >= nb[j]) break; gx[j][k] = __builtin_amdgcn_raw_buffer_load_b128(rw, vo, so[j][k], 16); }
              }
            int done;
            rec_reads(done, ml, xv, a_cnt, a_ml, a_x);
            const int dn = __builtin_amdgcn_readfirstlane(done);
            unsigned late = 0;
            if (want & 4) { late = (unsigned)__ballot(ext && stale(gm, tlb, thb) != 0); todo &= late ? 7 : 3; }
#pragma unroll
            for (int j = 0; j < 2; ++j)
              if (want & (1 << j)) {
                unsigned bad = 0;
#pragma unroll
                for (int k = 0; k < GK; ++k) { if (k >= nb[j]) break; bad |= stale(gx[j][k], tlb, thb); }
                todo &= __ballot(bad != 0) ? 7 : ~(1 << j);
              }
            if (!todo && (left || dn == ALL_DONE)) break;
            want = todo;
            if (__builtin_popcount(dn) < NSTR - 1) {
              want &= (late & ((1 << GK) - 1)) ? 6 : 7;
              want &= (late & (((1 << GK) - 1) << GK)) ? 5 : 7;
              if (want == 4) __builtin_amdgcn_s_sleep(LAZY);
              if (!want) __builtin_amdgcn_s_sleep(2);
            }
          }
          float mk = mine ? ml[0] : -FLT_MAX, lk = mine ? ml[1] : 0.f;
          mk = ext ? __uint_as_float(gm[0]) : mk; lk = ext ? __uint_as_float(gm[1]) : lk;
          const float Mn = row_max(fmaxf(mk, M));
          const float wt = __builtin_amdgcn_exp2f(mk - Mn), wc = __builtin_amdgcn_exp2f(M - Mn);
          M = Mn;
          L = __builtin_fmaf(L, wc, row_sum(wt * lk));
          o *= wc;
#pragma unroll
          for (int j = 0; j < 2; ++j)
#pragma unroll
            for (int k = 0; k < GK; ++k) o += rdl(wt, j * GK + k) * f32x2{__uint_as_float(gx[j][k][0]), __uint_as_float(gx[j][k][1])};
          if (!left) {
#pragma unroll
            for (int k = 0; k < NSTR; ++k) o += rdl(wt, 2 * GK + k) * xv[k];
          }
        } while (left);
        const int cx = __builtin_amdgcn_readlane(cxv, b);
        constexpr float MASKED = -998244352.0f * 1.4426950408889634f;
        if (cx < LK && MASKED - M > -200.f) {
          f32x2 sv = {0.f, 0.f};
          for (int t = cx; t < LK; ++t) {
            int ph = bt[b * NB + (t >> 4)];
            ph = ph < 0 ? ph + (int)nphys : ph;
            const unsigned vv = *(const __attribute__((address_space(1))) unsigned*)(vc + (size_t)ph * PAGE_BYTES + (t & 15) * 2048 + kvh * 256 + lane * 4);
            sv += f32x2{__uint_as_float(vv << 16), __uint_as_float(vv & 0xFFFF0000u)};
          }
          const float Mn = fmaxf(M, MASKED);
          const float a = __builtin_amdgcn_exp2f(M - Mn), wt = __builtin_amdgcn_exp2f(MASKED - Mn);
          L = L * a + wt * (float)(LK - cx);
          o = o * a + wt * sv;
        }
        const float inv = __builtin_amdgcn_rcpf(L);
        *(__attribute__((address_space(1))) unsigned*)(out + (b * H + kvh * G + r) * D + lane * 2) = cvtpk(o[0] * inv, o[1] * inv);
        if (tid == NSTR * 64) __hip_atomic_store((gu64p)(ws + WS_EPOCH) + kvh * B + b, ((unsigned long long)thb << 32) | tlb, __ATOMIC_RELAXED, __HIP_MEMORY_SCOPE_AGENT);
      }
    }
  }
}
}

extern "C" void kernel_launch(void* const* d_in, const int* in_sizes, int n_in, void* d_out, int out_size, void* d_ws, size_t ws_size,
                              hipStream_t stream) {
  attn_decode<<<256, NTHR, 0, stream>>>((const unsigned char*)d_in[0], (const unsigned char*)d_in[1], (const unsigned char*)d_in[2],
                                              (const int*)d_in[3], (const int*)d_in[4], (unsigned char*)d_ws, (unsigned short*)d_out,
                                              (long long)(in_sizes[1] / (PAGE * HKV * D)));
}
```

```cpp
#define PROBE 0
#include <hip/hip_runtime.h>
#include <hip/hip_bf16.h>
#include <stdint.h>
#include <float.h>

#define NSLOW 38
#define NS 2
#define SHR_A 8
#define OWN_B 0
#define WARM 1
#define LAZY 16

namespace {
constexpr int B = 8, H = 32, HKV = 8, G = 4, D = 128, PAGE = 16, NB = 512, LK = NB * PAGE;
constexpr int NFAST = (128 - NSLOW) / 3;
static_assert(NSLOW + 3 * NFAST == 128 && NSLOW < 64, "");
constexpr int NWAVE = 8, NTHR = NWAVE * 64, NSTR = 4;
constexpr int PAGE_BYTES = PAGE * HKV * D * 2;
constexpr float CSC = 0.088388347648318440f * 1.4426950408889634f;
constexpr int GK = 6;
constexpr int ROW = 528, RECW = 32 * ROW;
constexpr int L_TBL = 0;
constexpr int L_Q = L_TBL + B * NB * 4;
constexpr int L_ML = L_Q + 32 * 256;
constexpr int L_FM = L_ML + 32 * 32;
constexpr int L_CNT = L_FM + 32;
constexpr int L_RING = NS == 2 ? 32768 - 4096 : 32768;
constexpr int RINGW = NS * 8192;
constexpr int L_REC = NS == 2 ? L_RING + NSTR * RINGW : L_RING;
constexpr int RECS = NS == 2 ? RECW : RINGW;
constexpr int L_END = L_REC + NSTR * RECS;
static_assert(L_CNT + 4 <= L_RING && L_END <= 163840, "");
constexpr int WS_EPOCH = 0;
constexpr int AREA = B * 4096 + 1024;
constexpr int WS_PART = 4096;
constexpr int WS_BYTES = WS_PART + 256 * AREA;
constexpr unsigned long long TAG_STEP = 0x9E3779B97F4A7C15ull;

typedef __attribute__((ext_vector_type(4))) unsigned u32x4;
typedef __attribute__((ext_vector_type(2))) unsigned u32x2;
typedef __attribute__((ext_vector_type(4))) float f32x4;
typedef __attribute__((ext_vector_type(2))) float f32x2;
typedef __attribute__((ext_vector_type(8))) __bf16 bf16x8;
typedef __attribute__((ext_vector_type(4))) short s16x4;
typedef __attribute__((address_space(3))) unsigned char* ldsp;
typedef __attribute__((address_space(1))) unsigned long long* gu64p;

__device__ __forceinline__ unsigned cvtpk(float lo, float hi) {
  typedef __attribute__((ext_vector_type(2))) __bf16 bf16x2;
  bf16x2 r = {(__bf16)lo, (__bf16)hi};
  return __builtin_bit_cast(unsigned, r);
}
__device__ __forceinline__ float xg_max(float x) {
  auto s = __builtin_amdgcn_permlane16_swap(__float_as_uint(x), __float_as_uint(x), false, false);
  x = fmaxf(__uint_as_float(s[0]), __uint_as_float(s[1]));
  auto t = __builtin_amdgcn_permlane32_swap(__float_as_uint(x), __float_as_uint(x), false, false);
  return fmaxf(__uint_as_float(t[0]), __uint_as_float(t[1]));
}
__device__ __forceinline__ float xg_sum(float x) {
  auto s = __builtin_amdgcn_permlane16_swap(__float_as_uint(x), __float_as_uint(x), false, false);
  x = __uint_as_float(s[0]) + __uint_as_float(s[1]);
  auto t = __builtin_amdgcn_permlane32_swap(__float_as_uint(x), __float_as_uint(x), false, false);
  return __uint_as_float(t[0]) + __uint_as_float(t[1]);
}
template <int SH> __device__ __forceinline__ int row_shr(int x) { return __builtin_amdgcn_update_dpp(0, x, 0x110 + SH, 0xF, 0xF, true); }
template <int CTL> __device__ __forceinline__ float dpp(float x) { return __int_as_float(__builtin_amdgcn_update_dpp(0, __float_as_int(x), CTL, 0xF, 0xF, true)); }
#define DPP2(op, a, b) "s_nop 1\n\t" op " %0, %0, %0 " a " row_mask:0xf bank_mask:0xf\n\ts_nop 1\n\t" op " %0, %0, %0 " b " row_mask:0xf bank_mask:0xf\n\t"
__device__ __forceinline__ float quad_max(float x) { asm(DPP2("v_max_f32_dpp", "quad_perm:[1,0,3,2]", "quad_perm:[2,3,0,1]") : "+v"(x)); return x; }
__device__ __forceinline__ float quad_sum(float x) { asm(DPP2("v_add_f32_dpp", "quad_perm:[1,0,3,2]", "quad_perm:[2,3,0,1]") : "+v"(x)); return x; }
__device__ __forceinline__ float row_max(float x) { asm(DPP2("v_max_f32_dpp", "row_ror:8", "row_ror:4") DPP2("v_max_f32_dpp", "row_ror:2", "row_ror:1") : "+v"(x)); return x; }
__device__ __forceinline__ float row_sum(float x) { asm(DPP2("v_add_f32_dpp", "row_ror:8", "row_ror:4") DPP2("v_add_f32_dpp", "row_ror:2", "row_ror:1") : "+v"(x)); return x; }
__device__ __forceinline__ void rec_reads(int& done, f32x2& ml, f32x2 (&xv)[NSTR], int a_cnt, int a_ml, int a_x) {
  asm volatile("ds_read_b32 %0, %6\n\tds_read_b64 %1, %7\n\tds_read_b64 %2, %8\n\tds_read_b64 %3, %8 offset:%9\n\tds_read_b64 %4, %8 offset:%10\n\tds_read_b64 %5, %8 offset:%11\n\ts_waitcnt lgkmcnt(0)"
               : "=&v"(done), "=&v"(ml), "=&v"(xv[0]), "=&v"(xv[1]), "=&v"(xv[2]), "=&v"(xv[3]) : "v"(a_cnt), "v"(a_ml), "v"(a_x), "i"(RECS), "i"(2 * RECS), "i"(3 * RECS) : "memory");
}
constexpr int ALL_DONE = 0x01010101;
__device__ __forceinline__ float rdl(float x, int l) { return __int_as_float(__builtin_amdgcn_readlane(__float_as_int(x), l)); }
template <typename T> __device__ __forceinline__ T lds_ld(ldsp p, int off) { return *(__attribute__((address_space(3))) T*)(p + off); }
template <typename T> __device__ __forceinline__ T lds_vld(ldsp p, int off) { return *(volatile __attribute__((address_space(3))) T*)(p + off); }
template <typename T> __device__ __forceinline__ void lds_st(ldsp p, int off, T v) { *(__attribute__((address_space(3))) T*)(p + off) = v; }
template <typename T> __device__ __forceinline__ void lds_vst(ldsp p, int off, T v) { *(volatile __attribute__((address_space(3))) T*)(p + off) = v; }
__device__ __forceinline__ u32x4 seal(f32x2 v, unsigned tl, unsigned th) {
  return u32x4{__float_as_uint(v[0]), __float_as_uint(v[1]), __float_as_uint(v[0]) ^ tl, __float_as_uint(v[1]) ^ th};
}
__device__ __forceinline__ unsigned stale(u32x4 g, unsigned tl, unsigned th) { return (g[0] ^ g[2] ^ tl) | (g[1] ^ g[3] ^ th); }

#define STAMP(i) do { st_[i] = __builtin_amdgcn_s_memrealtime(); } while (0)
#define STAMP_OUT() do { if (w < NSTR) { const unsigned long long lim_ = st_[PROBE] + 4000; while (__builtin_amdgcn_s_memrealtime() < lim_) __builtin_amdgcn_s_sleep(1); } } while (0)
__device__ __forceinline__ void dma4(const int (&vo)[4], u32x4 rs, unsigned soff, unsigned ldsaddr) {
  asm volatile("s_mov_b32 m0, %6\n\ts_nop 4\n\t"
               "buffer_load_dwordx4 %0, %4, %5 offen nt lds\n\t"
               "buffer_load_dwordx4 %1, %4, %5 offen offset:1024 nt lds\n\t"
               "buffer_load_dwordx4 %2, %4, %5 offen offset:2048 nt lds\n\t"
               "buffer_load_dwordx4 %3, %4, %5 offen offset:3072 nt lds"
               :: "v"(vo[0]), "v"(vo[1]), "v"(vo[2]), "v"(vo[3]), "s"(rs), "s"(soff), "s"(ldsaddr) : "memory");
}

__global__ void __launch_bounds__(NTHR, 2)
attn_decode(const unsigned char* __restrict__ q, const unsigned char* kc, const unsigned char* vc,
            const int* __restrict__ bt, const int* __restrict__ ctxl, unsigned char* ws, unsigned short* out, long long nphys) {
  __shared__ __attribute__((aligned(16))) unsigned char smem[L_END];
  const ldsp lds = (ldsp)smem;
  unsigned long long st_[8] = {0, 0, 0, 0, 0, 0, 0, 0};
  STAMP(0);
  const int tid = threadIdx.x, lane = tid & 63;
  const int w = __builtin_amdgcn_readfirstlane(tid >> 6);
  const int g = lane >> 4, c = lane & 15;
  const int bx = blockIdx.x & 127, h4 = (bx >= NFAST) + (bx >= NFAST + NSLOW) + (bx >= 2 * NFAST + NSLOW);
  const int kvh = (blockIdx.x >> 7) * 4 + h4, nw = h4 == 1 ? NSLOW : NFAST;
  int wi = bx - h4 * NFAST - (h4 >= 2 ? NSLOW - NFAST : 0), rnw = h4 == 1 ? (65536 + NSLOW - 1) / NSLOW : (65536 + NFAST - 1) / NFAST;
  asm volatile("" : "+s"(kc), "+s"(vc), "+s"(out), "+s"(nphys), "+s"(wi), "+s"(rnw));
  if (w < NSTR) __builtin_amdgcn_s_setprio(3);
  int cxv = ctxl[lane & 7];
  int limv[2] = {ctxl[c >> 2], ctxl[4 + (c >> 2)]};
  int e[B];
#pragma unroll
  for (int b = 0; b < B; ++b) e[b] = bt[b * NB + tid];
  const u32x4 qv = *reinterpret_cast<const u32x4*>(q + (w * H + kvh * G) * D * 2 + lane * 16);
  unsigned long long tag = __hip_atomic_load((gu64p)(ws + WS_EPOCH) + kvh * B + ((lane >> 2) & 7), __ATOMIC_RELAXED, __HIP_MEMORY_SCOPE_AGENT);
  int kvo[4], vvo[4];
#pragma unroll
  for (int t = 0; t < 4; ++t) {
    const int row = 4 * t + g;
    kvo[t] = row * 2048 + ((c ^ row) * 16) + kvh * 256 - t * 1024;
    vvo[t] = row * 2048 + ((c ^ ((row & 7) << 1)) * 16) + kvh * 256 - t * 1024;
  }
  int fw = w + NSTR * lane;
  asm volatile("" : "+v"(kvo[0]), "+v"(kvo[1]), "+v"(kvo[2]), "+v"(kvo[3]), "+v"(vvo[0]), "+v"(vvo[1]), "+v"(vvo[2]), "+v"(vvo[3]), "+v"(fw));
  cxv = min(max(cxv, 0), LK);
#pragma unroll
  for (int x = 0; x < 2; ++x) limv[x] = min(max(limv[x], 0), LK) - 4 * g;
  asm volatile("" : "+v"(limv[0]), "+v"(limv[1]));
  int npv = (cxv + PAGE - 1) / PAGE;
  int npmax;
  {
    int t = max(npv, row_shr<1>(npv)); t = max(t, row_shr<2>(t)); t = max(t, row_shr<4>(t));
    npmax = __builtin_amdgcn_readlane(t, 7);
  }
  bool mis = false;
#pragma unroll
  for (int b = 0; b < B; ++b) { lds_st<int>(lds, L_TBL + (b * NB + tid) * 4, e[b]); mis |= (e[b] != e[0]); }
  {
    const unsigned long long bal = __ballot(mis);
    int first = min(bal ? (w * 64 + __builtin_ctzll(bal)) : NB, npmax);
    first = npmax ? first : 1;
    if (lane < 2) lds_st<int>(lds, lane ? L_CNT : L_FM + w * 4, lane ? 0 : first);
  }
  lds_st<u32x4>(lds, L_Q + (4 * w + g) * 256 + ((c ^ ((4 * w + g) & 15)) * 16), qv);
  tag += TAG_STEP;
  unsigned tl = (unsigned)tag, th = (unsigned)(tag >> 32);
  asm volatile("" : "+v"(tl), "+v"(th));
  __syncthreads();
  STAMP(1);
  int S;
  {
    const u32x4 f0 = lds_ld<u32x4>(lds, L_FM), f1 = lds_ld<u32x4>(lds, L_FM + 16);
    const unsigned s = min(min(min(f0[0], f0[1]), min(f0[2], f0[3])), min(min(f1[0], f1[1]), min(f1[2], f1[3])));
    S = __builtin_amdgcn_readfirstlane((int)s);
  }
  const int cnt = max(npv - S, 0);
  int se = cnt + row_shr<1>(cnt); se += row_shr<2>(se); se += row_shr<4>(se); se += S;
  const int sb = se - cnt;
  const int Wp = __builtin_amdgcn_readlane(se, 7);
  const int beta = (Wp * OWN_B) >> 12, ash = S * (SHR_A << 8);
  const int wr = ((((Wp - B * beta) << 6) + (ash >> 10)) * rnw) >> 6;
  const int mid = cnt ? sb + (cnt >> 1) : ((2 * (lane & 7) + 1) * Wp) >> 4;
  const int ymid = max(((mid - (lane & 7) * beta) << 16) + ash, 0);
  const int y0 = wi * wr, y1 = wi + 1 == nw ? 0x7FFFFFFF : y0 + wr;
  const int m0 = (int)(__ballot(ymid < y0) & 0xFF), m1 = (int)(__ballot(ymid < y1) & 0xFF);
  const int c0 = min(((y0 - min((y0 >> 8) * SHR_A, ash)) >> 16) + beta * __builtin_popcount(m0), Wp);
  const int c1 = min(((y1 - min((y1 >> 8) * SHR_A, ash)) >> 16) + beta * __builtin_popcount(m1), Wp);
  const int ownmask = m1 & ~m0;
  const int priv = (int)(__ballot(max(sb, c0) < min(se, c1)) & 0xFF);
  const int wgmask = (c0 < S && c0 < c1) ? 0xFF : priv;
  const auto rw = __builtin_amdgcn_make_buffer_rsrc(ws, 0, WS_BYTES, 0x00020000);
  const int n = (c1 - c0 - w + NSTR - 1) >> 2;
  if (w < NSTR) {
    unsigned dphys; int djb;
    {
      const int f = min(c0 + fw, Wp - 1);
      const int dl = S + (lane & 7) * NB - sb;
      int idx = f;
      for (int pm = priv; pm; pm &= pm - 1) {
        const int b = __builtin_ctz(pm);
        const int s0 = __builtin_amdgcn_readlane(sb, b), d0 = __builtin_amdgcn_readlane(dl, b);
        idx = f >= s0 ? f + d0 : idx;
      }
      int ph = lds_ld<int>(lds, L_TBL + (idx & (B * NB - 1)) * 4);
      ph = ph < 0 ? ph + (int)nphys : ph;
      dphys = (unsigned)ph * PAGE_BYTES;
      djb = ((idx & (NB - 1)) * PAGE) | (f < S ? 0xFF0000 : 0x10000 << ((idx >> 9) & 7));
    }
    const unsigned cbytes = (unsigned)nphys * PAGE_BYTES;
    const u32x4 rk = {(unsigned)(uintptr_t)kc, (unsigned)((uintptr_t)kc >> 32), cbytes, 0x00020000u};
    const u32x4 rv = {(unsigned)(uintptr_t)vc, (unsigned)((uintptr_t)vc >> 32), cbytes, 0x00020000u};
    const unsigned ring = (unsigned)(uintptr_t)lds + L_RING + w * RINGW;
#pragma unroll
    for (int u = 0; u < NS; ++u) {
      if (u >= n) break;
      const unsigned so = __builtin_amdgcn_readlane(dphys, u);
      dma4(kvo, rk, so, ring + u * 8192);
      dma4(vvo, rv, so, ring + u * 8192 + 4096);
    }
    STAMP(2);
    u32x4 qf[2][4];
#pragma unroll
    for (int x = 0; x < 2; ++x)
#pragma unroll
      for (int kk = 0; kk < 4; ++kk) qf[x][kk] = lds_ld<u32x4>(lds, L_Q + (x * 16 + c) * 256 + (((4 * kk + g) ^ c) * 16));
    int bbit[2];
#pragma unroll
    for (int x = 0; x < 2; ++x) bbit[x] = 1 << (16 + 4 * x + (c >> 2));
    int ka[4], va[8];
#pragma unroll
    for (int kk = 0; kk < 4; ++kk) ka[kk] = L_RING + w * RINGW + c * 256 + (((4 * kk + g) ^ c) * 16);
#pragma unroll
    for (int d = 0; d < 8; ++d) va[d] = L_RING + w * RINGW + 4096 + (4 * g + (c >> 2)) * 256 + ((((4 * g + (c >> 2)) & 7) ^ d) << 5) + ((c & 3) << 3);
    const int pub = wgmask & ~ownmask;
    const int lb = (int)(uintptr_t)lds;
    int b0 = __builtin_ctz(pub | 0x80);
    int a_rec = L_REC + w * RECS + c * ROW + g * 16, a_mlw = L_ML + c * 32 + w * 8, a_dn = L_CNT + w, one = 1;
    int a_cnt = lb + L_CNT, a_mlr = lb + L_ML + (((lane >> 2) & 7) * 4 + w) * 32 + (lane & 3) * 8, a_rd = L_REC + w * ROW + lane * 8, a_rd0 = lb + a_rd + b0 * 4 * ROW;
    int o_ml = (lane < 32 && (lane & 3) == 0 && ((pub >> (lane >> 2)) & 1)) ? WS_PART + blockIdx.x * AREA + B * 4096 + (lane + w) * 16 : WS_BYTES;
    int o_d = WS_PART + blockIdx.x * AREA + w * 1024 + lane * 16;
    unsigned tl0 = __builtin_amdgcn_readlane(tl, 4 * b0), th0 = __builtin_amdgcn_readlane(th, 4 * b0);
    int i0_ = 4 * b0, i1_ = i0_ + 1, i2_ = i0_ + 2, i3_ = i0_ + 3, so0 = b0 * 4096;
    asm volatile("" : "+v"(a_rec), "+v"(a_mlw), "+v"(a_mlr), "+v"(a_rd), "+v"(o_ml), "+v"(o_d), "+v"(a_dn), "+v"(one), "+v"(a_cnt), "+v"(a_rd0));
    asm volatile("" : "+s"(tl0), "+s"(th0), "+s"(i0_), "+s"(i1_), "+s"(i2_), "+s"(i3_), "+s"(so0));

    f32x4 acc[2][8];
    float m[2], l[2];
#pragma unroll
    for (int x = 0; x < 2; ++x) { m[x] = -FLT_MAX; l[x] = 0.f;
#pragma unroll
      for (int d = 0; d < 8; ++d) acc[x][d] = f32x4{0.f, 0.f, 0.f, 0.f}; }

    asm volatile("" : "+v"(qf[0][0]), "+v"(qf[0][1]), "+v"(qf[0][2]), "+v"(qf[0][3]), "+v"(qf[1][0]), "+v"(qf[1][1]), "+v"(qf[1][2]), "+v"(qf[1][3]));
    for (int i0 = 0; i0 < n; i0 += NS) {
#pragma unroll
      for (int u = 0; u < NS; ++u) {
        const int i = i0 + u;
        if (u && i >= n) break;
        const int jb = __builtin_amdgcn_readlane(djb, i);
        const unsigned so = __builtin_amdgcn_readlane(dphys, i + NS);
        const int rem = n - i;
        if (rem == 1) asm volatile("s_waitcnt vmcnt(4)" ::: "memory");
        if (NS == 3 && rem == 2) asm volatile("s_waitcnt vmcnt(12)" ::: "memory");
        asm volatile("s_waitcnt vmcnt(%0)" :: "i"(NS * 8 - 4) : "memory");
        if (i == 0) STAMP(3);
        u32x4 kf[4];
#pragma unroll
        for (int kk = 0; kk < 4; ++kk) kf[kk] = lds_ld<u32x4>(lds, ka[kk] + u * 8192);
        asm volatile("s_waitcnt lgkmcnt(0)" : "+v"(kf[0]), "+v"(kf[1]), "+v"(kf[2]), "+v"(kf[3]) :: "memory");
        if (rem > NS) dma4(kvo, rk, so, ring + u * 8192);
        u32x2 pb[2];
#pragma unroll
        for (int x = 0; x < 2; ++x) {
          if (jb & (0xF0000 << (4 * x))) {
            f32x4 s = {0.f, 0.f, 0.f, 0.f};
#pragma unroll
            for (int kk = 0; kk < 4; ++kk)
              s = __builtin_amdgcn_mfma_f32_16x16x32_bf16(__builtin_bit_cast(bf16x8, kf[kk]), __builtin_bit_cast(bf16x8, qf[x][kk]), s, 0, 0, 0);
            const int lim = (jb & bbit[x]) ? limv[x] - (jb & 0xFFFF) : 0;
#pragma unroll
            for (int r = 0; r < 4; ++r) s[r] = (r < lim) ? s[r] : -INFINITY;
            float mx = fmaxf(fmaxf(s[0], s[1]), fmaxf(s[2], s[3]));
            mx = xg_max(mx) * CSC;
            const float mn = fmaxf(m[x], mx);
            const float al = __builtin_amdgcn_exp2f(m[x] - mn);
            m[x] = mn;
            float p[4];
#pragma unroll
            for (int r = 0; r < 4; ++r) p[r] = __builtin_amdgcn_exp2f(__builtin_fmaf(s[r], CSC, -mn));
            l[x] = __builtin_fmaf(l[x], al, (p[0] + p[1]) + (p[2] + p[3]));
            pb[x] = u32x2{cvtpk(p[0], p[1]), cvtpk(p[2], p[3])};
#pragma unroll
            for (int d = 0; d < 8; ++d) acc[x][d] *= al;
          }
        }
        if (rem == 1) asm volatile("s_waitcnt vmcnt(0)" ::: "memory");
        if (rem == 2) asm volatile("s_waitcnt vmcnt(8)" ::: "memory");
        if (NS == 3 && rem == 3) asm volatile("s_waitcnt vmcnt(16)" ::: "memory");
        asm volatile("s_waitcnt vmcnt(%0)" :: "i"(NS * 8 - 4) : "memory");
        s16x4 vt[8];
#pragma unroll
        for (int d = 0; d < 8; ++d) {
          typedef __attribute__((ext_vector_type(4))) short v4i16;
          vt[d] = __builtin_amdgcn_ds_read_tr16_b64_v4i16((__attribute__((address_space(3))) v4i16*)(lds + (va[d] + u * 8192)));
        }
        asm volatile("s_waitcnt lgkmcnt(0)" : "+v"(vt[0]), "+v"(vt[1]), "+v"(vt[2]), "+v"(vt[3]), "+v"(vt[4]), "+v"(vt[5]), "+v"(vt[6]), "+v"(vt[7]) :: "memory");
        if (rem > NS) dma4(vvo, rv, so, ring + u * 8192 + 4096);
#pragma unroll
        for (int x = 0; x < 2; ++x) {
          if (jb & (0xF0000 << (4 * x))) {
#pragma unroll
            for (int d = 0; d < 8; ++d)
              acc[x][d] = __builtin_amdgcn_mfma_f32_16x16x16bf16_1k(vt[d], __builtin_bit_cast(s16x4, pb[x]), acc[x][d], 0, 0, 0);
          }
        }
      }
    }
    STAMP(4);
#pragma unroll
    for (int x = 0; x < 2; ++x) {
      if ((wgmask | ownmask) & (0xF << (4 * x))) {
#pragma unroll
        for (int d = 0; d < 8; ++d) lds_vst<f32x4>(lds, a_rec + x * 16 * ROW + d * 64, acc[x][d]);
        lds_vst<f32x2>(lds, a_mlw + x * 512, f32x2{m[x], xg_sum(l[x])});
      }
    }
    lds_vst<unsigned char>(lds, a_dn, (unsigned char)one);
    if (pub) {
      f32x2 ml, xv[NSTR];
#pragma unroll 1
      for (int spin = 0; spin < (1 << 20); ++spin) {
        int done;
        rec_reads(done, ml, xv, a_cnt, a_mlr, a_rd0);
        const int dn = __builtin_amdgcn_readfirstlane(done);
        if (dn == ALL_DONE) break;
        if (__builtin_popcount(dn) < NSTR - 1) __builtin_amdgcn_s_sleep(2);
      }
      if (wgmask == 0xFF) {
        f32x2 xa[B][NSTR];
#pragma unroll
        for (int b = 0; b < B; ++b)
#pragma unroll
          for (int k = 0; k < NSTR; ++k) xa[b][k] = lds_vld<f32x2>(lds, a_rd + b * 4 * ROW + k * RECS);
        const float M = quad_max(ml[0]);
        const float wt = __builtin_amdgcn_exp2f(ml[0] - M);
        __builtin_amdgcn_raw_buffer_store_b128(seal(f32x2{M, quad_sum(wt * ml[1])}, tl, th), rw, o_ml, 0, 16);
#pragma unroll
        for (int b = 0; b < B; ++b) {
          f32x2 o = rdl(wt, 4 * b) * xa[b][0];
#pragma unroll
          for (int k = 1; k < NSTR; ++k) o += rdl(wt, 4 * b + k) * xa[b][k];
          __builtin_amdgcn_raw_buffer_store_b128(seal(o, __builtin_amdgcn_readlane(tl, 4 * b), __builtin_amdgcn_readlane(th, 4 * b)), rw, o_d, b * 4096, 16);
        }
      } else {
        const float M = quad_max(ml[0]);
        const float wt = __builtin_amdgcn_exp2f(ml[0] - M);
        const f32x2 o0 = rdl(wt, i0_) * xv[0] + rdl(wt, i1_) * xv[1] + rdl(wt, i2_) * xv[2] + rdl(wt, i3_) * xv[3];
        __builtin_amdgcn_raw_buffer_store_b128(seal(o0, tl0, th0), rw, o_d, so0, 16);
        __builtin_amdgcn_raw_buffer_store_b128(seal(f32x2{M, quad_sum(wt * ml[1])}, tl, th), rw, o_ml, 0, 16);
        for (int pm = pub & (pub - 1); pm; pm &= pm - 1) {
          const int b = __builtin_ctz(pm);
          f32x2 o = rdl(wt, 4 * b) * lds_vld<f32x2>(lds, a_rd + b * 4 * ROW);
#pragma unroll
          for (int k = 1; k < NSTR; ++k) o += rdl(wt, 4 * b + k) * lds_vld<f32x2>(lds, a_rd + b * 4 * ROW + k * RECS);
          __builtin_amdgcn_raw_buffer_store_b128(seal(o, __builtin_amdgcn_readlane(tl, 4 * b), __builtin_amdgcn_readlane(th, 4 * b)), rw, o_d, b * 4096, 16);
        }
      }
    }
    STAMP(5);
  } else {
    const int r = w - NSTR, k16 = lane & 15;
    if (r == 0) { unsigned dummy; asm volatile("buffer_load_dword %0, off, %1, %2 sc1\n\ts_waitcnt vmcnt(0)" : "=v"(dummy) : "s"(rw), "s"(WS_PART + blockIdx.x * AREA + AREA - 128) : "memory"); }
    if (ownmask) {
      const int yj = lane * wr, yn = lane + 1 >= nw ? 0x7FFFFFFF : yj + wr;
      int cj = (yj - min((yj >> 8) * SHR_A, ash)) >> 16, cn = (yn - min((yn >> 8) * SHR_A, ash)) >> 16;
#pragma unroll
      for (int b = 0; b < B; ++b) { const int ym = __builtin_amdgcn_readlane(ymid, b); cj += ym < yj ? beta : 0; cn += ym < yn ? beta : 0; }
      cj = min(cj, Wp); cn = min(cn, Wp);
      const int hb = WS_PART + (blockIdx.x - wi) * AREA;
      const int vo = r * 1024 + lane * 16;
      int spin = 0;
      for (int om = ownmask; om; om &= om - 1) {
        const int b = __builtin_ctz(om), row = b * 4 + r;
        const unsigned tlb = __builtin_amdgcn_readlane(tl, 4 * b), thb = __builtin_amdgcn_readlane(th, 4 * b);
        const bool con = (cj < S && cj < cn) || max(cj, __builtin_amdgcn_readlane(sb, b)) < min(cn, __builtin_amdgcn_readlane(se, b));
        unsigned long long pend = __ballot(con && lane < nw && lane != wi);
        float M = -FLT_MAX, L = 0.f; f32x2 o = {0.f, 0.f};
        const int lb = (int)(uintptr_t)lds;
        const int a_cnt = lb + L_CNT, a_ml = lb + L_ML + row * 32 + (lane & 3) * 8, a_x = lb + L_REC + row * ROW + lane * 8;
        int left = __builtin_popcountll(pend);
        do {
          const int nr = left > 2 * GK ? min(left - 2 * GK, 2 * GK) : left;
          left -= nr;
          const int nb[2] = {max(nr - GK, 0), min(nr, GK)};
          int so[2][GK], vml = WS_BYTES;
#pragma unroll
          for (int j = 0; j < 2; ++j)
#pragma unroll
            for (int k = 0; k < GK; ++k) {
              so[j][k] = hb + __builtin_ctzll(pend | (1ull << 63)) * AREA + b * 4096;
              vml = (k16 == j * GK + k && k < nb[j]) ? so[j][k] + (B - b) * 4096 + row * 16 : vml;
              pend = k < nb[j] ? pend & (pend - 1) : pend;
            }
          const bool ext = vml != WS_BYTES, mine = !left && k16 >= 2 * GK;
          u32x4 gm, gx[2][GK];
#pragma unroll
          for (int j = 0; j < 2; ++j)
#pragma unroll
            for (int k = 0; k < GK; ++k) gx[j][k] = u32x4{0, 0, 0, 0};
          int todo = 7, want = 4;
          f32x2 ml, xv[NSTR];
#pragma unroll 1
          for (; spin < (1 << 18); ++spin) {
            if (want & 4) gm = __builtin_amdgcn_raw_buffer_load_b128(rw, vml, 0, 16);
#pragma unroll
            for (int j = 0; j < 2; ++j)
              if (want & (1 << j)) {
#pragma unroll
                for (int k = 0; k < GK; ++k) { if (k >= nb[j]) break; gx[j][k] = __builtin_amdgcn_raw_buffer_load_b128(rw, vo, so[j][k], 16); }
              }
            int done;
            rec_reads(done, ml, xv, a_cnt, a_ml, a_x);
            const int dn = __builtin_amdgcn_readfirstlane(done);
            unsigned late = 0;
            if (want & 4) { late = (unsigned)__ballot(ext && stale(gm, tlb, thb) != 0); todo &= late ? 7 : 3; }
#pragma unroll
            for (int j = 0; j < 2; ++j)
              if (want & (1 << j)) {
                unsigned bad = 0;
#pragma unroll
                for (int k = 0; k < GK; ++k) { if (k >= nb[j]) break; bad |= stale(gx[j][k], tlb, thb); }
                todo &= __ballot(bad != 0) ? 7 : ~(1 << j);
              }
            if (!todo && (left || dn == ALL_DONE)) break;
            want = todo;
            if (__builtin_popcount(dn) < NSTR - 1) {
              want &= (late & ((1 << GK) - 1)) ? 6 : 7;
              want &= (late & (((1 << GK) - 1) << GK)) ? 5 : 7;
              if (want == 4) __builtin_amdgcn_s_sleep(LAZY);
              if (!want) __builtin_amdgcn_s_sleep(2);
            }
          }
          float mk = mine ? ml[0] : -FLT_MAX, lk = mine ? ml[1] : 0.f;
          mk = ext ? __uint_as_float(gm[0]) : mk; lk = ext ? __uint_as_float(gm[1]) : lk;
          const float Mn = row_max(fmaxf(mk, M));
          const float wt = __builtin_amdgcn_exp2f(mk - Mn), wc = __builtin_amdgcn_exp2f(M - Mn);
          M = Mn;
          L = __builtin_fmaf(L, wc, row_sum(wt * lk));
          o *= wc;
#pragma unroll
          for (int j = 0; j < 2; ++j)
#pragma unroll
            for (int k = 0; k < GK; ++k) o += rdl(wt, j * GK + k) * f32x2{__uint_as_float(gx[j][k][0]), __uint_as_float(gx[j][k][1])};
          if (!left) {
#pragma unroll
            for (int k = 0; k < NSTR; ++k) o += rdl(wt, 2 * GK + k) * xv[k];
          }
        } while (left);
        const int cx = __builtin_amdgcn_readlane(cxv, b);
        constexpr float MASKED = -998244352.0f * 1.4426950408889634f;
        if (cx < LK && MASKED - M > -200.f) {
          f32x2 sv = {0.f, 0.f};
          for (int t = cx; t < LK; ++t) {
            int ph = bt[b * NB + (t >> 4)];
            ph = ph < 0 ? ph + (int)nphys : ph;
            const unsigned vv = *(const __attribute__((address_space(1))) unsigned*)(vc + (size_t)ph * PAGE_BYTES + (t & 15) * 2048 + kvh * 256 + lane * 4);
            sv += f32x2{__uint_as_float(vv << 16), __uint_as_float(vv & 0xFFFF0000u)};
          }
          const float Mn = fmaxf(M, MASKED);
          const float a = __builtin_amdgcn_exp2f(M - Mn), wt = __builtin_amdgcn_exp2f(MASKED - Mn);
          L = L * a + wt * (float)(LK - cx);
          o = o * a + wt * sv;
        }
        const float inv = __builtin_amdgcn_rcpf(L);
        *(__attribute__((address_space(1))) unsigned*)(out + (b * H + kvh * G + r) * D + lane * 2) = cvtpk(o[0] * inv, o[1] * inv);
        if (tid == NSTR * 64) __hip_atomic_store((gu64p)(ws + WS_EPOCH) + kvh * B + b, ((unsigned long long)thb << 32) | tlb, __ATOMIC_RELAXED, __HIP_MEMORY_SCOPE_AGENT);
      }
    }
  }
  STAMP(6);
  STAMP_OUT();
}
}

extern "C" void kernel_launch(void* const* d_in, const int* in_sizes, int n_in, void* d_out, int out_size, void* d_ws, size_t ws_size,
                              hipStream_t stream) {
  attn_decode<<<256, NTHR, 0, stream>>>((const unsigned char*)d_in[0], (const unsigned char*)d_in[1], (const unsigned char*)d_in[2],
                                              (const int*)d_in[3], (const int*)d_in[4], (unsigned char*)d_ws, (unsigned short*)d_out,
                                              (long long)(in_sizes[1] / (PAGE * HKV * D)));
}
```

```cpp
#include <hip/hip_runtime.h>
#include <hip/hip_bf16.h>
#include <stdint.h>
#include <float.h>

#define SHR_A 8
#define OWN_B 0
#define DESC 4
#define NEAR 2
#define LAZY 8
#define NBT 6
#define HSTART 0, 29, 69, 98, 127, 156, 196, 225, 256
#define NS 2
#define COMPM 0xF0000

namespace {
constexpr int B = 8, H = 32, HKV = 8, G = 4, D = 128, PAGE = 16, NB = 512, LK = NB * PAGE;
constexpr int HS[9] = {HSTART};
constexpr unsigned long long pack8(int cnt) { unsigned long long r = 0; for (int h = 0; h < 8; ++h) r |= (unsigned long long)(cnt ? HS[h + 1] - HS[h] : HS[h]) << (8 * h); return r; }
constexpr unsigned long long packr(int h0) { unsigned long long r = 0; for (int h = 0; h < 4; ++h) r |= (unsigned long long)((65535 + HS[h0 + h + 1] - HS[h0 + h]) / (HS[h0 + h + 1] - HS[h0 + h])) << (16 * h); return r; }
constexpr int NWAVE = 8, NTHR = NWAVE * 64, NSTR = 4;
constexpr int PAGE_BYTES = PAGE * HKV * D * 2;
constexpr float CSC = 0.088388347648318440f * 1.4426950408889634f;
constexpr int ROW = 528, RECW = 32 * ROW;
constexpr int L_TBL = 0;
constexpr int L_Q = L_TBL + B * NB * 4;
constexpr int L_ML = L_Q + 32 * 256;
constexpr int L_FM = L_ML + 32 * 32;
constexpr int L_CNT = L_FM + 32;
constexpr int L_RING = 32768 - 4096;
constexpr int RINGW = NS * 8192;
constexpr int L_REC = L_RING + NSTR * RINGW;
constexpr int RECS = RECW;
constexpr int L_END = L_REC + NSTR * RECS;
static_assert(L_CNT + 16 <= L_RING && L_END <= 163840, "");
constexpr int WS_EPOCH = 0;
constexpr int AREA = B * 4096 + 1024;
constexpr int WS_PART = 4096;
constexpr int WS_BYTES = WS_PART + 256 * AREA;
constexpr unsigned long long TAG_STEP = 0x9E3779B97F4A7C15ull;

typedef __attribute__((ext_vector_type(4))) unsigned u32x4;
typedef __attribute__((ext_vector_type(2))) unsigned u32x2;
typedef __attribute__((ext_vector_type(4))) float f32x4;
typedef __attribute__((ext_vector_type(2))) float f32x2;
typedef __attribute__((ext_vector_type(8))) __bf16 bf16x8;
typedef __attribute__((ext_vector_type(4))) short s16x4;
typedef __attribute__((address_space(3))) unsigned char* ldsp;
typedef __attribute__((address_space(1))) unsigned long long* gu64p;

__device__ __forceinline__ unsigned cvtpk(float lo, float hi) {
  typedef __attribute__((ext_vector_type(2))) __bf16 bf16x2;
  bf16x2 r = {(__bf16)lo, (__bf16)hi};
  return __builtin_bit_cast(unsigned, r);
}
__device__ __forceinline__ float xg_max(float x) {
  auto s = __builtin_amdgcn_permlane16_swap(__float_as_uint(x), __float_as_uint(x), false, false);
  x = fmaxf(__uint_as_float(s[0]), __uint_as_float(s[1]));
  auto t = __builtin_amdgcn_permlane32_swap(__float_as_uint(x), __float_as_uint(x), false, false);
  return fmaxf(__uint_as_float(t[0]), __uint_as_float(t[1]));
}
__device__ __forceinline__ float xg_sum(float x) {
  auto s = __builtin_amdgcn_permlane16_swap(__float_as_uint(x), __float_as_uint(x), false, false);
  x = __uint_as_float(s[0]) + __uint_as_float(s[1]);
  auto t = __builtin_amdgcn_permlane32_swap(__float_as_uint(x), __float_as_uint(x), false, false);
  return __uint_as_float(t[0]) + __uint_as_float(t[1]);
}
template <int SH> __device__ __forceinline__ int row_shr(int x) { return __builtin_amdgcn_update_dpp(0, x, 0x110 + SH, 0xF, 0xF, true); }
template <int CTL> __device__ __forceinline__ float dpp(float x) { return __int_as_float(__builtin_amdgcn_update_dpp(0, __float_as_int(x), CTL, 0xF, 0xF, true)); }
#define DPP2(op, a, b) "s_nop 1\n\t" op " %0, %0, %0 " a " row_mask:0xf bank_mask:0xf\n\ts_nop 1\n\t" op " %0, %0, %0 " b " row_mask:0xf bank_mask:0xf\n\t"
__device__ __forceinline__ float quad_max(float x) { asm(DPP2("v_max_f32_dpp", "quad_perm:[1,0,3,2]", "quad_perm:[2,3,0,1]") : "+v"(x)); return x; }
__device__ __forceinline__ float quad_sum(float x) { asm(DPP2("v_add_f32_dpp", "quad_perm:[1,0,3,2]", "quad_perm:[2,3,0,1]") : "+v"(x)); return x; }
__device__ __forceinline__ float row_max(float x) { asm(DPP2("v_max_f32_dpp", "row_ror:8", "row_ror:4") DPP2("v_max_f32_dpp", "row_ror:2", "row_ror:1") : "+v"(x)); return x; }
__device__ __forceinline__ float row_sum(float x) { asm(DPP2("v_add_f32_dpp", "row_ror:8", "row_ror:4") DPP2("v_add_f32_dpp", "row_ror:2", "row_ror:1") : "+v"(x)); return x; }
__device__ __forceinline__ void rec_reads(u32x2& fl, f32x2& ml, f32x2 (&xv)[NSTR], int a_cnt, int a_ml, int a_x) {
  asm volatile("ds_read_b64 %0, %6\n\tds_read_b64 %1, %7\n\tds_read_b64 %2, %8\n\tds_read_b64 %3, %8 offset:%9\n\tds_read_b64 %4, %8 offset:%10\n\tds_read_b64 %5, %8 offset:%11\n\ts_waitcnt lgkmcnt(0)"
               : "=&v"(fl), "=&v"(ml), "=&v"(xv[0]), "=&v"(xv[1]), "=&v"(xv[2]), "=&v"(xv[3]) : "v"(a_cnt), "v"(a_ml), "v"(a_x), "i"(RECS), "i"(2 * RECS), "i"(3 * RECS) : "memory");
}
constexpr int ALL_DONE = 0x01010101;
__device__ __forceinline__ float rdl(float x, int l) { return __int_as_float(__builtin_amdgcn_readlane(__float_as_int(x), l)); }
template <typename T> __device__ __forceinline__ T lds_ld(ldsp p, int off) { return *(__attribute__((address_space(3))) T*)(p + off); }
template <typename T> __device__ __forceinline__ T lds_vld(ldsp p, int off) { return *(volatile __attribute__((address_space(3))) T*)(p + off); }
template <typename T> __device__ __forceinline__ void lds_st(ldsp p, int off, T v) { *(__attribute__((address_space(3))) T*)(p + off) = v; }
template <typename T> __device__ __forceinline__ void lds_vst(ldsp p, int off, T v) { *(volatile __attribute__((address_space(3))) T*)(p + off) = v; }
__device__ __forceinline__ u32x4 seal(f32x2 v, unsigned tl, unsigned th) {
  return u32x4{__float_as_uint(v[0]), __float_as_uint(v[1]), __float_as_uint(v[0]) ^ tl, __float_as_uint(v[1]) ^ th};
}
__device__ __forceinline__ unsigned stale(u32x4 g, unsigned tl, unsigned th) { return (g[0] ^ g[2] ^ tl) | (g[1] ^ g[3] ^ th); }

#define STAMP(i)
#define STAMP_OUT()
__device__ __forceinline__ void dma4(const int (&vo)[4], u32x4 rs, unsigned soff, unsigned ldsaddr) {
  asm volatile("s_mov_b32 m0, %6\n\ts_nop 4\n\t"
               "buffer_load_dwordx4 %0, %4, %5 offen nt lds\n\t"
               "buffer_load_dwordx4 %1, %4, %5 offen offset:1024 nt lds\n\t"
               "buffer_load_dwordx4 %2, %4, %5 offen offset:2048 nt lds\n\t"
               "buffer_load_dwordx4 %3, %4, %5 offen offset:3072 nt lds"
               :: "v"(vo[0]), "v"(vo[1]), "v"(vo[2]), "v"(vo[3]), "s"(rs), "s"(soff), "s"(ldsaddr) : "memory");
}

__global__ void __launch_bounds__(NTHR, 2)
attn_decode(const unsigned char* __restrict__ q, const unsigned char* kc, const unsigned char* vc,
            const int* __restrict__ bt, const int* __restrict__ ctxl, unsigned char* ws, unsigned short* out, long long nphys) {
  __shared__ __attribute__((aligned(16))) unsigned char smem[L_END];
  const ldsp lds = (ldsp)smem;
  STAMP(0);
  const int tid = threadIdx.x, lane = tid & 63;
  const int w = __builtin_amdgcn_readfirstlane(tid >> 6);
  const int g = lane >> 4, c = lane & 15;
  int kvh = 0;
#pragma unroll
  for (int h = 1; h < HKV; ++h) kvh += blockIdx.x >= HS[h];
  const int hs = (int)(pack8(0) >> (8 * kvh)) & 255, nw = (int)(pack8(1) >> (8 * kvh)) & 255;
  int wi = blockIdx.x - hs, rnw = (int)((kvh < 4 ? packr(0) : packr(4)) >> (16 * (kvh & 3))) & 0xFFFF;
  asm volatile("" : "+s"(kc), "+s"(vc), "+s"(out), "+s"(nphys), "+s"(wi), "+s"(rnw));
  if (w < NSTR) __builtin_amdgcn_s_setprio(3);
  int cxv = ctxl[lane & 7];
  int limv[2] = {ctxl[c >> 2], ctxl[4 + (c >> 2)]};
  int e[B];
#pragma unroll
  for (int b = 0; b < B; ++b) e[b] = bt[b * NB + tid];
  const u32x4 qv = *reinterpret_cast<const u32x4*>(q + (w * H + kvh * G) * D * 2 + lane * 16);
  unsigned long long tag = __hip_atomic_load((gu64p)(ws + WS_EPOCH) + kvh * B + ((lane >> 2) & 7), __ATOMIC_RELAXED, __HIP_MEMORY_SCOPE_AGENT);
  int kvo[4], vvo[4];
#pragma unroll
  for (int t = 0; t < 4; ++t) {
    const int row = 4 * t + g;
    kvo[t] = row * 2048 + ((c ^ row) * 16) + kvh * 256 - t * 1024;
    vvo[t] = row * 2048 + ((c ^ ((row & 7) << 1)) * 16) + kvh * 256 - t * 1024;
  }
  int fw = w + NSTR * lane;
  asm volatile("" : "+v"(kvo[0]), "+v"(kvo[1]), "+v"(kvo[2]), "+v"(kvo[3]), "+v"(vvo[0]), "+v"(vvo[1]), "+v"(vvo[2]), "+v"(vvo[3]), "+v"(fw));
  cxv = min(max(cxv, 0), LK);
#pragma unroll
  for (int x = 0; x < 2; ++x) limv[x] = min(max(limv[x], 0), LK) - 4 * g;
  asm volatile("" : "+v"(limv[0]), "+v"(limv[1]));
  int npv = (cxv + PAGE - 1) / PAGE;
  int npmax;
  {
    int t = max(npv, row_shr<1>(npv)); t = max(t, row_shr<2>(t)); t = max(t, row_shr<4>(t));
    npmax = __builtin_amdgcn_readlane(t, 7);
  }
  bool mis = false;
#pragma unroll
  for (int b = 0; b < B; ++b) { lds_st<int>(lds, L_TBL + (b * NB + tid) * 4, e[b]); mis |= (e[b] != e[0]); }
  {
    const unsigned long long bal = __ballot(mis);
    int first = min(bal ? (w * 64 + __builtin_ctzll(bal)) : NB, npmax);
    first = npmax ? first : 1;
    if (lane < 4) lds_st<int>(lds, lane ? L_CNT - 4 + lane * 4 : L_FM + w * 4, lane ? 0 : first);
  }
  lds_st<u32x4>(lds, L_Q + (4 * w + g) * 256 + ((c ^ ((4 * w + g) & 15)) * 16), qv);
  tag += TAG_STEP;
  unsigned tl = (unsigned)tag, th = (unsigned)(tag >> 32);
  asm volatile("" : "+v"(tl), "+v"(th));
  __syncthreads();
  STAMP(1);
  int S;
  {
    const u32x4 f0 = lds_ld<u32x4>(lds, L_FM), f1 = lds_ld<u32x4>(lds, L_FM + 16);
    const unsigned s = min(min(min(f0[0], f0[1]), min(f0[2], f0[3])), min(min(f1[0], f1[1]), min(f1[2], f1[3])));
    S = __builtin_amdgcn_readfirstlane((int)s);
  }
  const int cnt = max(npv - S, 0);
  int se = cnt + row_shr<1>(cnt); se += row_shr<2>(se); se += row_shr<4>(se); se += S;
  const int sb = se - cnt;
  const int Wp = __builtin_amdgcn_readlane(se, 7);
  const int beta = (Wp * OWN_B) >> 12, ash = S * (SHR_A << 8);
  const int wr = ((((Wp - B * beta) << 6) + (ash >> 10)) * rnw) >> 6;
  const int mid = cnt ? sb + (cnt >> 1) : ((2 * (lane & 7) + 1) * Wp) >> 4;
  const int ymid = max(((mid - (lane & 7) * beta) << 16) + ash, 0);
  const int y0 = wi * wr, y1 = wi + 1 == nw ? 0x7FFFFFFF : y0 + wr;
  const int m0 = (int)(__ballot(ymid < y0) & 0xFF), m1 = (int)(__ballot(ymid < y1) & 0xFF);
  const int c0 = min(((y0 - min((y0 >> 8) * SHR_A, ash)) >> 16) + beta * __builtin_popcount(m0), Wp);
  const int c1 = min(((y1 - min((y1 >> 8) * SHR_A, ash)) >> 16) + beta * __builtin_popcount(m1), Wp);
  const int ownmask = m1 & ~m0;
  const int priv = (int)(__ballot(max(sb, c0) < min(se, c1)) & 0xFF);
  const int wgmask = (c0 < S && c0 < c1) ? 0xFF : priv;
  const auto rw = __builtin_amdgcn_make_buffer_rsrc(ws, 0, WS_BYTES, 0x00020000);
  const int n = (c1 - c0 - w + NSTR - 1) >> 2;
  if (__builtin_expect(w >= NSTR, 0)) {
    const int r = w - NSTR, lb = (int)(uintptr_t)lds, a_cnt = lb + L_CNT;
    const int pub = wgmask & ~ownmask;
    const int vo = r * 1024 + lane * 16;
    const int yj = lane * wr, yn = lane + 1 >= nw ? 0x7FFFFFFF : yj + wr;
    int cj = (yj - min((yj >> 8) * SHR_A, ash)) >> 16, cn = (yn - min((yn >> 8) * SHR_A, ash)) >> 16;
#pragma unroll
    for (int b = 0; b < B; ++b) { const int ym = __builtin_amdgcn_readlane(ymid, b); cj += ym < yj ? beta : 0; cn += ym < yn ? beta : 0; }
    cj = min(cj, Wp); cn = min(cn, Wp);
    const int hb = WS_PART + hs * AREA, mine = hb + wi * AREA;
    int spin = 0;
    if (pub) {
      const int b0 = __builtin_ctz(pub);
      int a_ml = lb + L_ML + (((lane >> 2) & 7) * 4 + r) * 32 + (lane & 3) * 8, a_rd = L_REC + r * ROW + lane * 8, a_x = lb + a_rd + b0 * 4 * ROW;
      int o_ml = (lane < 32 && (lane & 3) == 0 && ((pub >> (lane >> 2)) & 1)) ? mine + B * 4096 + (lane + r) * 16 : WS_BYTES;
      unsigned tl0 = __builtin_amdgcn_readlane(tl, 4 * b0), th0 = __builtin_amdgcn_readlane(th, 4 * b0);
      int so0 = mine + b0 * 4096;
      asm volatile("" : "+v"(a_ml), "+v"(a_rd), "+v"(a_x), "+v"(o_ml), "+s"(tl0), "+s"(th0), "+s"(so0));
      while (__builtin_amdgcn_readfirstlane(lds_vld<int>(lds, L_CNT + 8)) != ALL_DONE) __builtin_amdgcn_s_sleep(4);
      __builtin_amdgcn_raw_buffer_store_b128(seal(f32x2{0.f, -1.f}, tl, th), rw, o_ml, 0, 16);
      u32x2 fl; f32x2 ml, xv[NSTR];
#pragma unroll 1
      for (; spin < (1 << 20); ++spin) {
        rec_reads(fl, ml, xv, a_cnt, a_ml, a_x);
        if (__builtin_amdgcn_readfirstlane(fl[0]) == ALL_DONE) break;
        __builtin_amdgcn_s_sleep(1);
      }
      STAMP(2);
      const float e = __builtin_floorf(quad_max(ml[0]));
      const float wt = __builtin_amdgcn_exp2f(ml[0] - e);
      __builtin_amdgcn_raw_buffer_store_b128(seal(f32x2{e, quad_sum(wt * ml[1])}, tl, th), rw, o_ml, 0, 16);
      float w0 = rdl(wt, 4 * b0), w1 = rdl(wt, 4 * b0 + 1), w2 = rdl(wt, 4 * b0 + 2), w3 = rdl(wt, 4 * b0 + 3);
      asm volatile("" : "+s"(w0), "+s"(w1), "+s"(w2), "+s"(w3));
#pragma unroll 1
      for (; __builtin_amdgcn_readfirstlane(fl[1]) != ALL_DONE && spin < (1 << 21); ++spin) rec_reads(fl, ml, xv, a_cnt, a_ml, a_x);
      STAMP(3);
      __builtin_amdgcn_raw_buffer_store_b128(seal(w0 * xv[0] + w1 * xv[1] + w2 * xv[2] + w3 * xv[3], tl0, th0), rw, vo, so0, 16);
      if (pub == 0xFF) {
        f32x2 xa[B][NSTR];
#pragma unroll
        for (int b = 1; b < B; ++b)
#pragma unroll
          for (int k = 0; k < NSTR; ++k) xa[b][k] = lds_vld<f32x2>(lds, a_rd + b * 4 * ROW + k * RECS);
#pragma unroll
        for (int b = 1; b < B; ++b) {
          f32x2 o = rdl(wt, 4 * b) * xa[b][0];
#pragma unroll
          for (int k = 1; k < NSTR; ++k) o += rdl(wt, 4 * b + k) * xa[b][k];
          __builtin_amdgcn_raw_buffer_store_b128(seal(o, __builtin_amdgcn_readlane(tl, 4 * b), __builtin_amdgcn_readlane(th, 4 * b)), rw, vo, mine + b * 4096, 16);
        }
      } else {
        for (int pm = pub & (pub - 1); pm; pm &= pm - 1) {
          const int b = __builtin_ctz(pm);
          f32x2 o = rdl(wt, 4 * b) * lds_vld<f32x2>(lds, a_rd + b * 4 * ROW);
#pragma unroll
          for (int k = 1; k < NSTR; ++k) o += rdl(wt, 4 * b + k) * lds_vld<f32x2>(lds, a_rd + b * 4 * ROW + k * RECS);
          __builtin_amdgcn_raw_buffer_store_b128(seal(o, __builtin_amdgcn_readlane(tl, 4 * b), __builtin_amdgcn_readlane(th, 4 * b)), rw, vo, mine + b * 4096, 16);
        }
      }
      STAMP(5);
    }
    for (int om = ownmask; om; om &= om - 1) {
      const int b = __builtin_ctz(om), row = b * 4 + r;
      const unsigned tlb = __builtin_amdgcn_readlane(tl, 4 * b), thb = __builtin_amdgcn_readlane(th, 4 * b);
      const bool con = (cj < S && cj < cn) || max(cj, __builtin_amdgcn_readlane(sb, b)) < min(cn, __builtin_amdgcn_readlane(se, b));
      int vml = (con && lane < nw && lane != wi) ? hb + lane * AREA + B * 4096 + row * 16 : WS_BYTES;
      unsigned long long pa = __ballot(vml != WS_BYTES), pw = pa, pb = 0, pn = 0;
      const int sd = hb + b * 4096, cx = __builtin_amdgcn_readlane(cxv, b);
      int a_ml = lb + L_ML + row * 32 + (lane & 3) * 8, a_x = lb + L_REC + row * ROW + lane * 8;
      unsigned short* op = out + (b * H + kvh * G + r) * D + lane * 2;
      float ej = -FLT_MAX, lj = 0.f, E = -FLT_MAX, Lx = 0.f, sc = 0.f, w0 = 0.f, w1 = 0.f, w2 = 0.f, w3 = 0.f;
      int wh = 0;
      double s0 = 0., s1 = 0.;
      f32x2 o = {0.f, 0.f};
      int st = 0;
      asm volatile("" : "+v"(vml), "+v"(a_ml), "+v"(a_x), "+v"(op));
      while (__builtin_amdgcn_readfirstlane(lds_vld<int>(lds, L_CNT + 8)) == 0) __builtin_amdgcn_s_sleep(8);
#pragma unroll 1
      for (; spin < (1 << 21); ++spin) {
        u32x4 gm, gx[NBT];
        int kk[NBT];
        unsigned long long t = pb;
#pragma unroll
        for (int j = 0; j < NBT; ++j) {
          if (!t) break;
          kk[j] = __builtin_ctzll(t); t &= t - 1;
          gx[j] = __builtin_amdgcn_raw_buffer_load_b128(rw, vo, sd + kk[j] * AREA, 16);
        }
        if (pa) gm = __builtin_amdgcn_raw_buffer_load_b128(rw, vml, 0, 16);
        u32x2 fl; f32x2 ml, xv[NSTR];
        rec_reads(fl, ml, xv, a_cnt, a_ml, a_x);
        bool idle = false;
        if (pa) {
          const bool ok = vml != WS_BYTES && stale(gm, tlb, thb) == 0, fin = ok && (int)gm[1] >= 0;
          const unsigned long long nv = __ballot(ok) & pw, nf = __ballot(fin);
          pw &= ~nv; pn = nv;
          idle = !pb && !nv;
          if (nf) {
            ej = fin ? __uint_as_float(gm[0]) : ej; lj = fin ? __uint_as_float(gm[1]) : lj;
            vml = fin ? WS_BYTES : vml;
            pa &= ~nf;
            const float En = xg_max(row_max(ej));
            const int dj = (int)fmaxf(ej - En, -1023.f), dE = (int)fmaxf(E - En, -1023.f);
            wh = (dj + 1023) << 20;
            Lx = xg_sum(row_sum(__builtin_amdgcn_ldexpf(lj, dj)));
            s0 = __builtin_amdgcn_ldexp(s0, dE); s1 = __builtin_amdgcn_ldexp(s1, dE);
            E = En;
            STAMP(2);
          }
        }
        if (!(st & 2) && !pa && __builtin_amdgcn_readfirstlane(fl[0]) == ALL_DONE) {
          float M = fmaxf(E, quad_max(ml[0])), L = 0.f;
          constexpr float MASKED = -998244352.0f * 1.4426950408889634f;
          if (__builtin_expect(cx < LK && MASKED - M > -200.f, 0)) {
            for (int p = cx; p < LK; ++p) {
              int ph = bt[b * NB + (p >> 4)];
              ph = ph < 0 ? ph + (int)nphys : ph;
              const unsigned vv = *(const __attribute__((address_space(1))) unsigned*)(vc + (size_t)ph * PAGE_BYTES + (p & 15) * 2048 + kvh * 256 + lane * 4);
              o += f32x2{__uint_as_float(vv << 16), __uint_as_float(vv & 0xFFFF0000u)};
            }
            M = fmaxf(M, MASKED);
            const float wm = __builtin_amdgcn_exp2f(MASKED - M);
            L = wm * (float)(LK - cx);
            o *= wm;
          }
          const float a = __builtin_amdgcn_exp2f(E - M);
          float wt = __builtin_amdgcn_exp2f(ml[0] - M);
          const float inv = __builtin_amdgcn_rcpf(L + __builtin_fmaf(Lx, a, quad_sum(wt * ml[1])));
          sc = a * inv; wt *= inv; o *= inv;
          w0 = rdl(wt, 0); w1 = rdl(wt, 1); w2 = rdl(wt, 2); w3 = rdl(wt, 3);
          st |= 2;
        }
        if (st == 2 && __builtin_amdgcn_readfirstlane(fl[1]) == ALL_DONE) {
          o += w0 * xv[0]; o += w1 * xv[1]; o += w2 * xv[2]; o += w3 * xv[3];
          st = 6;
        }
        t = pb;
#pragma unroll
        for (int j = 0; j < NBT; ++j) {
          if (!t) break;
          t &= t - 1;
          if (!__ballot(stale(gx[j], tlb, thb) != 0) && !((pa >> kk[j]) & 1)) {
            const double wk = __hiloint2double(__builtin_amdgcn_readlane(wh, kk[j]), 0);
            s0 = __builtin_fma((double)__uint_as_float(gx[j][0]), wk, s0);
            s1 = __builtin_fma((double)__uint_as_float(gx[j][1]), wk, s1);
            pb &= ~(1ull << kk[j]);
          }
        }
        pb |= pn; pn = 0;
        if (!pb && st == 6) break;
        if (idle && __builtin_amdgcn_readfirstlane(fl[1]) != ALL_DONE) __builtin_amdgcn_s_sleep(LAZY);
      }
      STAMP(3);
      const unsigned res = cvtpk(__builtin_fmaf((float)s0, sc, o[0]), __builtin_fmaf((float)s1, sc, o[1]));
      asm volatile("global_store_dword %0, %1, off sc0 sc1" :: "v"(op), "v"(res) : "memory");
      if (tid == NSTR * 64) __hip_atomic_store((gu64p)(ws + WS_EPOCH) + kvh * B + b, ((unsigned long long)thb << 32) | tlb, __ATOMIC_RELAXED, __HIP_MEMORY_SCOPE_AGENT);
    }
    STAMP(6);
    STAMP_OUT();
    __builtin_amdgcn_s_waitcnt(0);
    __builtin_amdgcn_endpgm();
  }
  {
    unsigned dphys; int djb;
    {
      const int f = (wi & DESC) ? max(c1 - 1 - fw, 0) : min(c0 + fw, Wp - 1);
      const int dl = S + (lane & 7) * NB - sb;
      int idx = f;
      for (int pm = priv; pm; pm &= pm - 1) {
        const int b = __builtin_ctz(pm);
        const int s0 = __builtin_amdgcn_readlane(sb, b), d0 = __builtin_amdgcn_readlane(dl, b);
        idx = f >= s0 ? f + d0 : idx;
      }
      int ph = lds_ld<int>(lds, L_TBL + (idx & (B * NB - 1)) * 4);
      ph = ph < 0 ? ph + (int)nphys : ph;
      dphys = (unsigned)ph * PAGE_BYTES;
      djb = ((idx & (NB - 1)) * PAGE) | (f < S ? 0xFF0000 : 0x10000 << ((idx >> 9) & 7));
    }
    const unsigned cbytes = (unsigned)nphys * PAGE_BYTES;
    const u32x4 rk = {(unsigned)(uintptr_t)kc, (unsigned)((uintptr_t)kc >> 32), cbytes, 0x00020000u};
    const u32x4 rv = {(unsigned)(uintptr_t)vc, (unsigned)((uintptr_t)vc >> 32), cbytes, 0x00020000u};
    const unsigned ring = (unsigned)(uintptr_t)lds + L_RING + w * RINGW;
#pragma unroll
    for (int u = 0; u < NS; ++u) {
      if (u >= n) break;
      const unsigned so = __builtin_amdgcn_readlane(dphys, u);
      dma4(kvo, rk, so, ring + u * 8192);
      dma4(vvo, rv, so, ring + u * 8192 + 4096);
    }
    STAMP(2);
    u32x4 qf[2][4];
#pragma unroll
    for (int x = 0; x < 2; ++x)
#pragma unroll
      for (int kk = 0; kk < 4; ++kk) qf[x][kk] = lds_ld<u32x4>(lds, L_Q + (x * 16 + c) * 256 + (((4 * kk + g) ^ c) * 16));
    int bbit[2];
#pragma unroll
    for (int x = 0; x < 2; ++x) bbit[x] = 1 << (16 + 4 * x + (c >> 2));
    int ka[4], va[8];
#pragma unroll
    for (int kk = 0; kk < 4; ++kk) ka[kk] = L_RING + w * RINGW + c * 256 + (((4 * kk + g) ^ c) * 16);
#pragma unroll
    for (int d = 0; d < 8; ++d) va[d] = L_RING + w * RINGW + 4096 + (4 * g + (c >> 2)) * 256 + ((((4 * g + (c >> 2)) & 7) ^ d) << 5) + ((c & 3) << 3);
    int a_rec = L_REC + w * RECS + c * ROW + g * 16, a_mlw = L_ML + c * 32 + w * 8, a_fl = L_CNT + w, one = 1;
    asm volatile("" : "+v"(a_rec), "+v"(a_mlw), "+v"(a_fl), "+v"(one));

    f32x4 acc[2][8];
    float m[2], l[2];
#pragma unroll
    for (int x = 0; x < 2; ++x) { m[x] = -FLT_MAX; l[x] = 0.f;
#pragma unroll
      for (int d = 0; d < 8; ++d) acc[x][d] = f32x4{0.f, 0.f, 0.f, 0.f}; }

    asm volatile("" : "+v"(qf[0][0]), "+v"(qf[0][1]), "+v"(qf[0][2]), "+v"(qf[0][3]), "+v"(qf[1][0]), "+v"(qf[1][1]), "+v"(qf[1][2]), "+v"(qf[1][3]));
    for (int i0 = 0; i0 < n; i0 += NS) {
#pragma unroll
      for (int u = 0; u < NS; ++u) {
        const int i = i0 + u;
        if (u && i >= n) break;
        const int jb = __builtin_amdgcn_readlane(djb, i);
        const unsigned so = __builtin_amdgcn_readlane(dphys, i + NS);
        const int rem = n - i;
        if (rem == 1) asm volatile("s_waitcnt vmcnt(4)" ::: "memory");
        asm volatile("s_waitcnt vmcnt(%0)" :: "i"(NS * 8 - 4) : "memory");
        if (i == 0) STAMP(3);
        u32x4 kf[4];
#pragma unroll
        for (int kk = 0; kk < 4; ++kk) kf[kk] = lds_ld<u32x4>(lds, ka[kk] + u * 8192);
        asm volatile("s_waitcnt lgkmcnt(0)" : "+v"(kf[0]), "+v"(kf[1]), "+v"(kf[2]), "+v"(kf[3]) :: "memory");
        if (rem > NS) dma4(kvo, rk, so, ring + u * 8192);
        u32x2 pb[2];
#pragma unroll
        for (int x = 0; x < 2; ++x) {
          if (jb & (COMPM << (4 * x))) {
            f32x4 s = {0.f, 0.f, 0.f, 0.f};
#pragma unroll
            for (int kk = 0; kk < 4; ++kk)
              s = __builtin_amdgcn_mfma_f32_16x16x32_bf16(__builtin_bit_cast(bf16x8, kf[kk]), __builtin_bit_cast(bf16x8, qf[x][kk]), s, 0, 0, 0);
            const int lim = (jb & bbit[x]) ? limv[x] - (jb & 0xFFFF) : 0;
#pragma unroll
            for (int r = 0; r < 4; ++r) s[r] = (r < lim) ? s[r] : -INFINITY;
            float mx = fmaxf(fmaxf(s[0], s[1]), fmaxf(s[2], s[3]));
            mx = xg_max(mx) * CSC;
            const float mn = fmaxf(m[x], mx);
            const float al = __builtin_amdgcn_exp2f(m[x] - mn);
            m[x] = mn;
            float p[4];
#pragma unroll
            for (int r = 0; r < 4; ++r) p[r] = __builtin_amdgcn_exp2f(__builtin_fmaf(s[r], CSC, -mn));
            l[x] = __builtin_fmaf(l[x], al, (p[0] + p[1]) + (p[2] + p[3]));
            pb[x] = u32x2{cvtpk(p[0], p[1]), cvtpk(p[2], p[3])};
#pragma unroll
            for (int d = 0; d < 8; ++d) acc[x][d] *= al;
          }
        }
        if (rem <= NEAR) {
          lds_vst<unsigned char>(lds, a_fl + 8, (unsigned char)one);
          if (rem == 1) {
#pragma unroll
            for (int x = 0; x < 2; ++x) lds_vst<f32x2>(lds, a_mlw + x * 512, f32x2{m[x], xg_sum(l[x])});
            lds_vst<unsigned char>(lds, a_fl, (unsigned char)one);
            asm volatile("s_waitcnt vmcnt(0)" ::: "memory");
          }
          if (rem == 2) asm volatile("s_waitcnt vmcnt(8)" ::: "memory");
        }
        asm volatile("s_waitcnt vmcnt(%0)" :: "i"(NS * 8 - 4) : "memory");
        s16x4 vt[8];
#pragma unroll
        for (int d = 0; d < 8; ++d) {
          typedef __attribute__((ext_vector_type(4))) short v4i16;
          vt[d] = __builtin_amdgcn_ds_read_tr16_b64_v4i16((__attribute__((address_space(3))) v4i16*)(lds + (va[d] + u * 8192)));
        }
        asm volatile("s_waitcnt lgkmcnt(0)" : "+v"(vt[0]), "+v"(vt[1]), "+v"(vt[2]), "+v"(vt[3]), "+v"(vt[4]), "+v"(vt[5]), "+v"(vt[6]), "+v"(vt[7]) :: "memory");
        if (rem > NS) dma4(vvo, rv, so, ring + u * 8192 + 4096);
#pragma unroll
        for (int x = 0; x < 2; ++x) {
          if (jb & (COMPM << (4 * x))) {
#pragma unroll
            for (int d = 0; d < 8; ++d)
              acc[x][d] = __builtin_amdgcn_mfma_f32_16x16x16bf16_1k(vt[d], __builtin_bit_cast(s16x4, pb[x]), acc[x][d], 0, 0, 0);
          }
        }
      }
    }
    STAMP(4);
#pragma unroll
    for (int x = 0; x < 2; ++x) {
      if ((wgmask | ownmask) & (0xF << (4 * x))) {
#pragma unroll
        for (int d = 0; d < 8; ++d) lds_vst<f32x4>(lds, a_rec + x * 16 * ROW + d * 64, acc[x][d]);
      }
    }
    if (n <= 0) {
#pragma unroll
      for (int x = 0; x < 2; ++x) lds_vst<f32x2>(lds, a_mlw + x * 512, f32x2{-FLT_MAX, 0.f});
      lds_vst<unsigned char>(lds, a_fl + 8, (unsigned char)one);
      lds_vst<unsigned char>(lds, a_fl, (unsigned char)one);
    }
    lds_vst<unsigned char>(lds, a_fl + 4, (unsigned char)one);
  }
  STAMP(6);
  STAMP_OUT();
}
}

extern "C" void kernel_launch(void* const* d_in, const int* in_sizes, int n_in, void* d_out, int out_size, void* d_ws, size_t ws_size,
                              hipStream_t stream) {
  attn_decode<<<256, NTHR, 0, stream>>>((const unsigned char*)d_in[0], (const unsigned char*)d_in[1], (const unsigned char*)d_in[2],
                                              (const int*)d_in[3], (const int*)d_in[4], (unsigned char*)d_ws, (unsigned short*)d_out,
                                              (long long)(in_sizes[1] / (PAGE * HKV * D)));
}
```

```cpp
#define PROBE 0
#include <hip/hip_runtime.h>
#include <hip/hip_bf16.h>
#include <stdint.h>
#include <float.h>

#define SHR_A 8
#define OWN_B 0
#define DESC 4
#define NEAR 2
#define LAZY 8
#define NBT 6
#define HSTART 0, 29, 69, 98, 127, 156, 196, 225, 256
#define NS 2
#define COMPM 0xF0000

namespace {
constexpr int B = 8, H = 32, HKV = 8, G = 4, D = 128, PAGE = 16, NB = 512, LK = NB * PAGE;
constexpr int HS[9] = {HSTART};
constexpr unsigned long long pack8(int cnt) { unsigned long long r = 0; for (int h = 0; h < 8; ++h) r |= (unsigned long long)(cnt ? HS[h + 1] - HS[h] : HS[h]) << (8 * h); return r; }
constexpr unsigned long long packr(int h0) { unsigned long long r = 0; for (int h = 0; h < 4; ++h) r |= (unsigned long long)((65535 + HS[h0 + h + 1] - HS[h0 + h]) / (HS[h0 + h + 1] - HS[h0 + h])) << (16 * h); return r; }
constexpr int NWAVE = 8, NTHR = NWAVE * 64, NSTR = 4;
constexpr int PAGE_BYTES = PAGE * HKV * D * 2;
constexpr float CSC = 0.088388347648318440f * 1.4426950408889634f;
constexpr int ROW = 528, RECW = 32 * ROW;
constexpr int L_TBL = 0;
constexpr int L_Q = L_TBL + B * NB * 4;
constexpr int L_ML = L_Q + 32 * 256;
constexpr int L_FM = L_ML + 32 * 32;
constexpr int L_CNT = L_FM + 32;
constexpr int L_RING = 32768 - 4096;
constexpr int RINGW = NS * 8192;
constexpr int L_REC = L_RING + NSTR * RINGW;
constexpr int RECS = RECW;
constexpr int L_END = L_REC + NSTR * RECS;
static_assert(L_CNT + 16 <= L_RING && L_END <= 163840, "");
constexpr int WS_EPOCH = 0;
constexpr int AREA = B * 4096 + 1024;
constexpr int WS_PART = 4096;
constexpr int WS_BYTES = WS_PART + 256 * AREA;
constexpr unsigned long long TAG_STEP = 0x9E3779B97F4A7C15ull;

typedef __attribute__((ext_vector_type(4))) unsigned u32x4;
typedef __attribute__((ext_vector_type(2))) unsigned u32x2;
typedef __attribute__((ext_vector_type(4))) float f32x4;
typedef __attribute__((ext_vector_type(2))) float f32x2;
typedef __attribute__((ext_vector_type(8))) __bf16 bf16x8;
typedef __attribute__((ext_vector_type(4))) short s16x4;
typedef __attribute__((address_space(3))) unsigned char* ldsp;
typedef __attribute__((address_space(1))) unsigned long long* gu64p;

__device__ __forceinline__ unsigned cvtpk(float lo, float hi) {
  typedef __attribute__((ext_vector_type(2))) __bf16 bf16x2;
  bf16x2 r = {(__bf16)lo, (__bf16)hi};
  return __builtin_bit_cast(unsigned, r);
}
__device__ __forceinline__ float xg_max(float x) {
  auto s = __builtin_amdgcn_permlane16_swap(__float_as_uint(x), __float_as_uint(x), false, false);
  x = fmaxf(__uint_as_float(s[0]), __uint_as_float(s[1]));
  auto t = __builtin_amdgcn_permlane32_swap(__float_as_uint(x), __float_as_uint(x), false, false);
  return fmaxf(__uint_as_float(t[0]), __uint_as_float(t[1]));
}
__device__ __forceinline__ float xg_sum(float x) {
  auto s = __builtin_amdgcn_permlane16_swap(__float_as_uint(x), __float_as_uint(x), false, false);
  x = __uint_as_float(s[0]) + __uint_as_float(s[1]);
  auto t = __builtin_amdgcn_permlane32_swap(__float_as_uint(x), __float_as_uint(x), false, false);
  return __uint_as_float(t[0]) + __uint_as_float(t[1]);
}
template <int SH> __device__ __forceinline__ int row_shr(int x) { return __builtin_amdgcn_update_dpp(0, x, 0x110 + SH, 0xF, 0xF, true); }
template <int CTL> __device__ __forceinline__ float dpp(float x) { return __int_as_float(__builtin_amdgcn_update_dpp(0, __float_as_int(x), CTL, 0xF, 0xF, true)); }
#define DPP2(op, a, b) "s_nop 1\n\t" op " %0, %0, %0 " a " row_mask:0xf bank_mask:0xf\n\ts_nop 1\n\t" op " %0, %0, %0 " b " row_mask:0xf bank_mask:0xf\n\t"
__device__ __forceinline__ float quad_max(float x) { asm(DPP2("v_max_f32_dpp", "quad_perm:[1,0,3,2]", "quad_perm:[2,3,0,1]") : "+v"(x)); return x; }
__device__ __forceinline__ float quad_sum(float x) { asm(DPP2("v_add_f32_dpp", "quad_perm:[1,0,3,2]", "quad_perm:[2,3,0,1]") : "+v"(x)); return x; }
__device__ __forceinline__ float row_max(float x) { asm(DPP2("v_max_f32_dpp", "row_ror:8", "row_ror:4") DPP2("v_max_f32_dpp", "row_ror:2", "row_ror:1") : "+v"(x)); return x; }
__device__ __forceinline__ float row_sum(float x) { asm(DPP2("v_add_f32_dpp", "row_ror:8", "row_ror:4") DPP2("v_add_f32_dpp", "row_ror:2", "row_ror:1") : "+v"(x)); return x; }
__device__ __forceinline__ void rec_reads(u32x2& fl, f32x2& ml, f32x2 (&xv)[NSTR], int a_cnt, int a_ml, int a_x) {
  asm volatile("ds_read_b64 %0, %6\n\tds_read_b64 %1, %7\n\tds_read_b64 %2, %8\n\tds_read_b64 %3, %8 offset:%9\n\tds_read_b64 %4, %8 offset:%10\n\tds_read_b64 %5, %8 offset:%11\n\ts_waitcnt lgkmcnt(0)"
               : "=&v"(fl), "=&v"(ml), "=&v"(xv[0]), "=&v"(xv[1]), "=&v"(xv[2]), "=&v"(xv[3]) : "v"(a_cnt), "v"(a_ml), "v"(a_x), "i"(RECS), "i"(2 * RECS), "i"(3 * RECS) : "memory");
}
constexpr int ALL_DONE = 0x01010101;
__device__ __forceinline__ float rdl(float x, int l) { return __int_as_float(__builtin_amdgcn_readlane(__float_as_int(x), l)); }
template <typename T> __device__ __forceinline__ T lds_ld(ldsp p, int off) { return *(__attribute__((address_space(3))) T*)(p + off); }
template <typename T> __device__ __forceinline__ T lds_vld(ldsp p, int off) { return *(volatile __attribute__((address_space(3))) T*)(p + off); }
template <typename T> __device__ __forceinline__ void lds_st(ldsp p, int off, T v) { *(__attribute__((address_space(3))) T*)(p + off) = v; }
template <typename T> __device__ __forceinline__ void lds_vst(ldsp p, int off, T v) { *(volatile __attribute__((address_space(3))) T*)(p + off) = v; }
__device__ __forceinline__ u32x4 seal(f32x2 v, unsigned tl, unsigned th) {
  return u32x4{__float_as_uint(v[0]), __float_as_uint(v[1]), __float_as_uint(v[0]) ^ tl, __float_as_uint(v[1]) ^ th};
}
__device__ __forceinline__ unsigned stale(u32x4 g, unsigned tl, unsigned th) { return (g[0] ^ g[2] ^ tl) | (g[1] ^ g[3] ^ th); }

#define STAMP(i) do { st_[i] = __builtin_amdgcn_s_memrealtime(); } while (0)
#define STAMP_OUT() do { if ((PROBE & 8) ? w >= NSTR : w < NSTR) { const unsigned long long lim_ = st_[PROBE & 7] + 4000; while (__builtin_amdgcn_s_memrealtime() < lim_) __builtin_amdgcn_s_sleep(1); } } while (0)
__device__ __forceinline__ void dma4(const int (&vo)[4], u32x4 rs, unsigned soff, unsigned ldsaddr) {
  asm volatile("s_mov_b32 m0, %6\n\ts_nop 4\n\t"
               "buffer_load_dwordx4 %0, %4, %5 offen nt lds\n\t"
               "buffer_load_dwordx4 %1, %4, %5 offen offset:1024 nt lds\n\t"
               "buffer_load_dwordx4 %2, %4, %5 offen offset:2048 nt lds\n\t"
               "buffer_load_dwordx4 %3, %4, %5 offen offset:3072 nt lds"
               :: "v"(vo[0]), "v"(vo[1]), "v"(vo[2]), "v"(vo[3]), "s"(rs), "s"(soff), "s"(ldsaddr) : "memory");
}

__global__ void __launch_bounds__(NTHR, 2)
attn_decode(const unsigned char* __restrict__ q, const unsigned char* kc, const unsigned char* vc,
            const int* __restrict__ bt, const int* __restrict__ ctxl, unsigned char* ws, unsigned short* out, long long nphys) {
  __shared__ __attribute__((aligned(16))) unsigned char smem[L_END];
  const ldsp lds = (ldsp)smem;
  unsigned long long st_[8] = {0, 0, 0, 0, 0, 0, 0, 0};
  STAMP(0);
  const int tid = threadIdx.x, lane = tid & 63;
  const int w = __builtin_amdgcn_readfirstlane(tid >> 6);
  const int g = lane >> 4, c = lane & 15;
  int kvh = 0;
#pragma unroll
  for (int h = 1; h < HKV; ++h) kvh += blockIdx.x >= HS[h];
  const int hs = (int)(pack8(0) >> (8 * kvh)) & 255, nw = (int)(pack8(1) >> (8 * kvh)) & 255;
  int wi = blockIdx.x - hs, rnw = (int)((kvh < 4 ? packr(0) : packr(4)) >> (16 * (kvh & 3))) & 0xFFFF;
  asm volatile("" : "+s"(kc), "+s"(vc), "+s"(out), "+s"(nphys), "+s"(wi), "+s"(rnw));
  if (w < NSTR) __builtin_amdgcn_s_setprio(3);
  int cxv = ctxl[lane & 7];
  int limv[2] = {ctxl[c >> 2], ctxl[4 + (c >> 2)]};
  int e[B];
#pragma unroll
  for (int b = 0; b < B; ++b) e[b] = bt[b * NB + tid];
  const u32x4 qv = *reinterpret_cast<const u32x4*>(q + (w * H + kvh * G) * D * 2 + lane * 16);
  unsigned long long tag = __hip_atomic_load((gu64p)(ws + WS_EPOCH) + kvh * B + ((lane >> 2) & 7), __ATOMIC_RELAXED, __HIP_MEMORY_SCOPE_AGENT);
  int kvo[4], vvo[4];
#pragma unroll
  for (int t = 0; t < 4; ++t) {
    const int row = 4 * t + g;
    kvo[t] = row * 2048 + ((c ^ row) * 16) + kvh * 256 - t * 1024;
    vvo[t] = row * 2048 + ((c ^ ((row & 7) << 1)) * 16) + kvh * 256 - t * 1024;
  }
  int fw = w + NSTR * lane;
  asm volatile("" : "+v"(kvo[0]), "+v"(kvo[1]), "+v"(kvo[2]), "+v"(kvo[3]), "+v"(vvo[0]), "+v"(vvo[1]), "+v"(vvo[2]), "+v"(vvo[3]), "+v"(fw));
  cxv = min(max(cxv, 0), LK);
#pragma unroll
  for (int x = 0; x < 2; ++x) limv[x] = min(max(limv[x], 0), LK) - 4 * g;
  asm volatile("" : "+v"(limv[0]), "+v"(limv[1]));
  int npv = (cxv + PAGE - 1) / PAGE;
  int npmax;
  {
    int t = max(npv, row_shr<1>(npv)); t = max(t, row_shr<2>(t)); t = max(t, row_shr<4>(t));
    npmax = __builtin_amdgcn_readlane(t, 7);
  }
  bool mis = false;
#pragma unroll
  for (int b = 0; b < B; ++b) { lds_st<int>(lds, L_TBL + (b * NB + tid) * 4, e[b]); mis |= (e[b] != e[0]); }
  {
    const unsigned long long bal = __ballot(mis);
    int first = min(bal ? (w * 64 + __builtin_ctzll(bal)) : NB, npmax);
    first = npmax ? first : 1;
    if (lane < 4) lds_st<int>(lds, lane ? L_CNT - 4 + lane * 4 : L_FM + w * 4, lane ? 0 : first);
  }
  lds_st<u32x4>(lds, L_Q + (4 * w + g) * 256 + ((c ^ ((4 * w + g) & 15)) * 16), qv);
  tag += TAG_STEP;
  unsigned tl = (unsigned)tag, th = (unsigned)(tag >> 32);
  asm volatile("" : "+v"(tl), "+v"(th));
  __syncthreads();
  STAMP(1);
  int S;
  {
    const u32x4 f0 = lds_ld<u32x4>(lds, L_FM), f1 = lds_ld<u32x4>(lds, L_FM + 16);
    const unsigned s = min(min(min(f0[0], f0[1]), min(f0[2], f0[3])), min(min(f1[0], f1[1]), min(f1[2], f1[3])));
    S = __builtin_amdgcn_readfirstlane((int)s);
  }
  const int cnt = max(npv - S, 0);
  int se = cnt + row_shr<1>(cnt); se += row_shr<2>(se); se += row_shr<4>(se); se += S;
  const int sb = se - cnt;
  const int Wp = __builtin_amdgcn_readlane(se, 7);
  const int beta = (Wp * OWN_B) >> 12, ash = S * (SHR_A << 8);
  const int wr = ((((Wp - B * beta) << 6) + (ash >> 10)) * rnw) >> 6;
  const int mid = cnt ? sb + (cnt >> 1) : ((2 * (lane & 7) + 1) * Wp) >> 4;
  const int ymid = max(((mid - (lane & 7) * beta) << 16) + ash, 0);
  const int y0 = wi * wr, y1 = wi + 1 == nw ? 0x7FFFFFFF : y0 + wr;
  const int m0 = (int)(__ballot(ymid < y0) & 0xFF), m1 = (int)(__ballot(ymid < y1) & 0xFF);
  const int c0 = min(((y0 - min((y0 >> 8) * SHR_A, ash)) >> 16) + beta * __builtin_popcount(m0), Wp);
  const int c1 = min(((y1 - min((y1 >> 8) * SHR_A, ash)) >> 16) + beta * __builtin_popcount(m1), Wp);
  const int ownmask = m1 & ~m0;
  const int priv = (int)(__ballot(max(sb, c0) < min(se, c1)) & 0xFF);
  const int wgmask = (c0 < S && c0 < c1) ? 0xFF : priv;
  const auto rw = __builtin_amdgcn_make_buffer_rsrc(ws, 0, WS_BYTES, 0x00020000);
  const int n = (c1 - c0 - w + NSTR - 1) >> 2;
  if (__builtin_expect(w >= NSTR, 0)) {
    const int r = w - NSTR, lb = (int)(uintptr_t)lds, a_cnt = lb + L_CNT;
    const int pub = wgmask & ~ownmask;
    const int vo = r * 1024 + lane * 16;
    const int yj = lane * wr, yn = lane + 1 >= nw ? 0x7FFFFFFF : yj + wr;
    int cj = (yj - min((yj >> 8) * SHR_A, ash)) >> 16, cn = (yn - min((yn >> 8) * SHR_A, ash)) >> 16;
#pragma unroll
    for (int b = 0; b < B; ++b) { const int ym = __builtin_amdgcn_readlane(ymid, b); cj += ym < yj ? beta : 0; cn += ym < yn ? beta : 0; }
    cj = min(cj, Wp); cn = min(cn, Wp);
    const int hb = WS_PART + hs * AREA, mine = hb + wi * AREA;
    int spin = 0;
    if (pub) {
      const int b0 = __builtin_ctz(pub);
      int a_ml = lb + L_ML + (((lane >> 2) & 7) * 4 + r) * 32 + (lane & 3) * 8, a_rd = L_REC + r * ROW + lane * 8, a_x = lb + a_rd + b0 * 4 * ROW;
      int o_ml = (lane < 32 && (lane & 3) == 0 && ((pub >> (lane >> 2)) & 1)) ? mine + B * 4096 + (lane + r) * 16 : WS_BYTES;
      unsigned tl0 = __builtin_amdgcn_readlane(tl, 4 * b0), th0 = __builtin_amdgcn_readlane(th, 4 * b0);
      int so0 = mine + b0 * 4096;
      asm volatile("" : "+v"(a_ml), "+v"(a_rd), "+v"(a_x), "+v"(o_ml), "+s"(tl0), "+s"(th0), "+s"(so0));
      while (__builtin_amdgcn_readfirstlane(lds_vld<int>(lds, L_CNT + 8)) != ALL_DONE) __builtin_amdgcn_s_sleep(4);
      __builtin_amdgcn_raw_buffer_store_b128(seal(f32x2{0.f, -1.f}, tl, th), rw, o_ml, 0, 16);
      u32x2 fl; f32x2 ml, xv[NSTR];
#pragma unroll 1
      for (; spin < (1 << 20); ++spin) {
        rec_reads(fl, ml, xv, a_cnt, a_ml, a_x);
        if (__builtin_amdgcn_readfirstlane(fl[0]) == ALL_DONE) break;
        __builtin_amdgcn_s_sleep(1);
      }
      STAMP(2);
      const float e = __builtin_floorf(quad_max(ml[0]));
      const float wt = __builtin_amdgcn_exp2f(ml[0] - e);
      __builtin_amdgcn_raw_buffer_store_b128(seal(f32x2{e, quad_sum(wt * ml[1])}, tl, th), rw, o_ml, 0, 16);
      float w0 = rdl(wt, 4 * b0), w1 = rdl(wt, 4 * b0 + 1), w2 = rdl(wt, 4 * b0 + 2), w3 = rdl(wt, 4 * b0 + 3);
      asm volatile("" : "+s"(w0), "+s"(w1), "+s"(w2), "+s"(w3));
#pragma unroll 1
      for (; __builtin_amdgcn_readfirstlane(fl[1]) != ALL_DONE && spin < (1 << 21); ++spin) rec_reads(fl, ml, xv, a_cnt, a_ml, a_x);
      STAMP(3);
      __builtin_amdgcn_raw_buffer_store_b128(seal(w0 * xv[0] + w1 * xv[1] + w2 * xv[2] + w3 * xv[3], tl0, th0), rw, vo, so0, 16);
      if (pub == 0xFF) {
        f32x2 xa[B][NSTR];
#pragma unroll
        for (int b = 1; b < B; ++b)
#pragma unroll
          for (int k = 0; k < NSTR; ++k) xa[b][k] = lds_vld<f32x2>(lds, a_rd + b * 4 * ROW + k * RECS);
#pragma unroll
        for (int b = 1; b < B; ++b) {
          f32x2 o = rdl(wt, 4 * b) * xa[b][0];
#pragma unroll
          for (int k = 1; k < NSTR; ++k) o += rdl(wt, 4 * b + k) * xa[b][k];
          __builtin_amdgcn_raw_buffer_store_b128(seal(o, __builtin_amdgcn_readlane(tl, 4 * b), __builtin_amdgcn_readlane(th, 4 * b)), rw, vo, mine + b * 4096, 16);
        }
      } else {
        for (int pm = pub & (pub - 1); pm; pm &= pm - 1) {
          const int b = __builtin_ctz(pm);
          f32x2 o = rdl(wt, 4 * b) * lds_vld<f32x2>(lds, a_rd + b * 4 * ROW);
#pragma unroll
          for (int k = 1; k < NSTR; ++k) o += rdl(wt, 4 * b + k) * lds_vld<f32x2>(lds, a_rd + b * 4 * ROW + k * RECS);
          __builtin_amdgcn_raw_buffer_store_b128(seal(o, __builtin_amdgcn_readlane(tl, 4 * b), __builtin_amdgcn_readlane(th, 4 * b)), rw, vo, mine + b * 4096, 16);
        }
      }
      STAMP(5);
    }
    for (int om = ownmask; om; om &= om - 1) {
      const int b = __builtin_ctz(om), row = b * 4 + r;
      const unsigned tlb = __builtin_amdgcn_readlane(tl, 4 * b), thb = __builtin_amdgcn_readlane(th, 4 * b);
      const bool con = (cj < S && cj < cn) || max(cj, __builtin_amdgcn_readlane(sb, b)) < min(cn, __builtin_amdgcn_readlane(se, b));
      int vml = (con && lane < nw && lane != wi) ? hb + lane * AREA + B * 4096 + row * 16 : WS_BYTES;
      unsigned long long pa = __ballot(vml != WS_BYTES), pw = pa, pb = 0, pn = 0;
      const int sd = hb + b * 4096, cx = __builtin_amdgcn_readlane(cxv, b);
      int a_ml = lb + L_ML + row * 32 + (lane & 3) * 8, a_x = lb + L_REC + row * ROW + lane * 8;
      unsigned short* op = out + (b * H + kvh * G + r) * D + lane * 2;
      float ej = -FLT_MAX, lj = 0.f, E = -FLT_MAX, Lx = 0.f, sc = 0.f, w0 = 0.f, w1 = 0.f, w2 = 0.f, w3 = 0.f;
      int wh = 0;
      double s0 = 0., s1 = 0.;
      f32x2 o = {0.f, 0.f};
      int st = 0;
      asm volatile("" : "+v"(vml), "+v"(a_ml), "+v"(a_x), "+v"(op));
      while (__builtin_amdgcn_readfirstlane(lds_vld<int>(lds, L_CNT + 8)) == 0) __builtin_amdgcn_s_sleep(8);
#pragma unroll 1
      for (; spin < (1 << 21); ++spin) {
        u32x4 gm, gx[NBT];
        int kk[NBT];
        unsigned long long t = pb;
#pragma unroll
        for (int j = 0; j < NBT; ++j) {
          if (!t) break;
          kk[j] = __builtin_ctzll(t); t &= t - 1;
          gx[j] = __builtin_amdgcn_raw_buffer_load_b128(rw, vo, sd + kk[j] * AREA, 16);
        }
        if (pa) gm = __builtin_amdgcn_raw_buffer_load_b128(rw, vml, 0, 16);
        u32x2 fl; f32x2 ml, xv[NSTR];
        rec_reads(fl, ml, xv, a_cnt, a_ml, a_x);
        bool idle = false;
        if (pa) {
          const bool ok = vml != WS_BYTES && stale(gm, tlb, thb) == 0, fin = ok && (int)gm[1] >= 0;
          const unsigned long long nv = __ballot(ok) & pw, nf = __ballot(fin);
          pw &= ~nv; pn = nv;
          idle = !pb && !nv;
          if (nf) {
            ej = fin ? __uint_as_float(gm[0]) : ej; lj = fin ? __uint_as_float(gm[1]) : lj;
            vml = fin ? WS_BYTES : vml;
            pa &= ~nf;
            const float En = xg_max(row_max(ej));
            const int dj = (int)fmaxf(ej - En, -1023.f), dE = (int)fmaxf(E - En, -1023.f);
            wh = (dj + 1023) << 20;
            Lx = xg_sum(row_sum(__builtin_amdgcn_ldexpf(lj, dj)));
            s0 = __builtin_amdgcn_ldexp(s0, dE); s1 = __builtin_amdgcn_ldexp(s1, dE);
            E = En;
            STAMP(2);
          }
        }
        if (!(st & 2) && !pa && __builtin_amdgcn_readfirstlane(fl[0]) == ALL_DONE) {
          float M = fmaxf(E, quad_max(ml[0])), L = 0.f;
          constexpr float MASKED = -998244352.0f * 1.4426950408889634f;
          if (__builtin_expect(cx < LK && MASKED - M > -200.f, 0)) {
            for (int p = cx; p < LK; ++p) {
              int ph = bt[b * NB + (p >> 4)];
              ph = ph < 0 ? ph + (int)nphys : ph;
              const unsigned vv = *(const __attribute__((address_space(1))) unsigned*)(vc + (size_t)ph * PAGE_BYTES + (p & 15) * 2048 + kvh * 256 + lane * 4);
              o += f32x2{__uint_as_float(vv << 16), __uint_as_float(vv & 0xFFFF0000u)};
            }
            M = fmaxf(M, MASKED);
            const float wm = __builtin_amdgcn_exp2f(MASKED - M);
            L = wm * (float)(LK - cx);
            o *= wm;
          }
          const float a = __builtin_amdgcn_exp2f(E - M);
          float wt = __builtin_amdgcn_exp2f(ml[0] - M);
          const float inv = __builtin_amdgcn_rcpf(L + __builtin_fmaf(Lx, a, quad_sum(wt * ml[1])));
          sc = a * inv; wt *= inv; o *= inv;
          w0 = rdl(wt, 0); w1 = rdl(wt, 1); w2 = rdl(wt, 2); w3 = rdl(wt, 3);
          st |= 2;
        }
        if (st == 2 && __builtin_amdgcn_readfirstlane(fl[1]) == ALL_DONE) {
          o += w0 * xv[0]; o += w1 * xv[1]; o += w2 * xv[2]; o += w3 * xv[3];
          st = 6;
        }
        t = pb;
#pragma unroll
        for (int j = 0; j < NBT; ++j) {
          if (!t) break;
          t &= t - 1;
          if (!__ballot(stale(gx[j], tlb, thb) != 0) && !((pa >> kk[j]) & 1)) {
            const double wk = __hiloint2double(__builtin_amdgcn_readlane(wh, kk[j]), 0);
            s0 = __builtin_fma((double)__uint_as_float(gx[j][0]), wk, s0);
            s1 = __builtin_fma((double)__uint_as_float(gx[j][1]), wk, s1);
            pb &= ~(1ull << kk[j]);
          }
        }
        pb |= pn; pn = 0;
        if (!pb && st == 6) break;
        if (idle && __builtin_amdgcn_readfirstlane(fl[1]) != ALL_DONE) __builtin_amdgcn_s_sleep(LAZY);
      }
      STAMP(3);
      const unsigned res = cvtpk(__builtin_fmaf((float)s0, sc, o[0]), __builtin_fmaf((float)s1, sc, o[1]));
      asm volatile("global_store_dword %0, %1, off sc0 sc1" :: "v"(op), "v"(res) : "memory");
      if (tid == NSTR * 64) __hip_atomic_store((gu64p)(ws + WS_EPOCH) + kvh * B + b, ((unsigned long long)thb << 32) | tlb, __ATOMIC_RELAXED, __HIP_MEMORY_SCOPE_AGENT);
    }
    STAMP(6);
    STAMP_OUT();
    __builtin_amdgcn_s_waitcnt(0);
    __builtin_amdgcn_endpgm();
  }
  {
    unsigned dphys; int djb;
    {
      const int f = (wi & DESC) ? max(c1 - 1 - fw, 0) : min(c0 + fw, Wp - 1);
      const int dl = S + (lane & 7) * NB - sb;
      int idx = f;
      for (int pm = priv; pm; pm &= pm - 1) {
        const int b = __builtin_ctz(pm);
        const int s0 = __builtin_amdgcn_readlane(sb, b), d0 = __builtin_amdgcn_readlane(dl, b);
        idx = f >= s0 ? f + d0 : idx;
      }
      int ph = lds_ld<int>(lds, L_TBL + (idx & (B * NB - 1)) * 4);
      ph = ph < 0 ? ph + (int)nphys : ph;
      dphys = (unsigned)ph * PAGE_BYTES;
      djb = ((idx & (NB - 1)) * PAGE) | (f < S ? 0xFF0000 : 0x10000 << ((idx >> 9) & 7));
    }
    const unsigned cbytes = (unsigned)nphys * PAGE_BYTES;
    const u32x4 rk = {(unsigned)(uintptr_t)kc, (unsigned)((uintptr_t)kc >> 32), cbytes, 0x00020000u};
    const u32x4 rv = {(unsigned)(uintptr_t)vc, (unsigned)((uintptr_t)vc >> 32), cbytes, 0x00020000u};
    const unsigned ring = (unsigned)(uintptr_t)lds + L_RING + w * RINGW;
#pragma unroll
    for (int u = 0; u < NS; ++u) {
      if (u >= n) break;
      const unsigned so = __builtin_amdgcn_readlane(dphys, u);
      dma4(kvo, rk, so, ring + u * 8192);
      dma4(vvo, rv, so, ring + u * 8192 + 4096);
    }
    STAMP(2);
    u32x4 qf[2][4];
#pragma unroll
    for (int x = 0; x < 2; ++x)
#pragma unroll
      for (int kk = 0; kk < 4; ++kk) qf[x][kk] = lds_ld<u32x4>(lds, L_Q + (x * 16 + c) * 256 + (((4 * kk + g) ^ c) * 16));
    int bbit[2];
#pragma unroll
    for (int x = 0; x < 2; ++x) bbit[x] = 1 << (16 + 4 * x + (c >> 2));
    int ka[4], va[8];
#pragma unroll
    for (int kk = 0; kk < 4; ++kk) ka[kk] = L_RING + w * RINGW + c * 256 + (((4 * kk + g) ^ c) * 16);
#pragma unroll
    for (int d = 0; d < 8; ++d) va[d] = L_RING + w * RINGW + 4096 + (4 * g + (c >> 2)) * 256 + ((((4 * g + (c >> 2)) & 7) ^ d) << 5) + ((c & 3) << 3);
    int a_rec = L_REC + w * RECS + c * ROW + g * 16, a_mlw = L_ML + c * 32 + w * 8, a_fl = L_CNT + w, one = 1;
    asm volatile("" : "+v"(a_rec), "+v"(a_mlw), "+v"(a_fl), "+v"(one));

    f32x4 acc[2][8];
    float m[2], l[2];
#pragma unroll
    for (int x = 0; x < 2; ++x) { m[x] = -FLT_MAX; l[x] = 0.f;
#pragma unroll
      for (int d = 0; d < 8; ++d) acc[x][d] = f32x4{0.f, 0.f, 0.f, 0.f}; }

    asm volatile("" : "+v"(qf[0][0]), "+v"(qf[0][1]), "+v"(qf[0][2]), "+v"(qf[0][3]), "+v"(qf[1][0]), "+v"(qf[1][1]), "+v"(qf[1][2]), "+v"(qf[1][3]));
    for (int i0 = 0; i0 < n; i0 += NS) {
#pragma unroll
      for (int u = 0; u < NS; ++u) {
        const int i = i0 + u;
        if (u && i >= n) break;
        const int jb = __builtin_amdgcn_readlane(djb, i);
        const unsigned so = __builtin_amdgcn_readlane(dphys, i + NS);
        const int rem = n - i;
        if (rem == 1) asm volatile("s_waitcnt vmcnt(4)" ::: "memory");
        asm volatile("s_waitcnt vmcnt(%0)" :: "i"(NS * 8 - 4) : "memory");
        if (i == 0) STAMP(3);
        u32x4 kf[4];
#pragma unroll
        for (int kk = 0; kk < 4; ++kk) kf[kk] = lds_ld<u32x4>(lds, ka[kk] + u * 8192);
        asm volatile("s_waitcnt lgkmcnt(0)" : "+v"(kf[0]), "+v"(kf[1]), "+v"(kf[2]), "+v"(kf[3]) :: "memory");
        if (rem > NS) dma4(kvo, rk, so, ring + u * 8192);
        u32x2 pb[2];
#pragma unroll
        for (int x = 0; x < 2; ++x) {
          if (jb & (COMPM << (4 * x))) {
            f32x4 s = {0.f, 0.f, 0.f, 0.f};
#pragma unroll
            for (int kk = 0; kk < 4; ++kk)
              s = __builtin_amdgcn_mfma_f32_16x16x32_bf16(__builtin_bit_cast(bf16x8, kf[kk]), __builtin_bit_cast(bf16x8, qf[x][kk]), s, 0, 0, 0);
            const int lim = (jb & bbit[x]) ? limv[x] - (jb & 0xFFFF) : 0;
#pragma unroll
            for (int r = 0; r < 4; ++r) s[r] = (r < lim) ? s[r] : -INFINITY;
            float mx = fmaxf(fmaxf(s[0], s[1]), fmaxf(s[2], s[3]));
            mx = xg_max(mx) * CSC;
            const float mn = fmaxf(m[x], mx);
            const float al = __builtin_amdgcn_exp2f(m[x] - mn);
            m[x] = mn;
            float p[4];
#pragma unroll
            for (int r = 0; r < 4; ++r) p[r] = __builtin_amdgcn_exp2f(__builtin_fmaf(s[r], CSC, -mn));
            l[x] = __builtin_fmaf(l[x], al, (p[0] + p[1]) + (p[2] + p[3]));
            pb[x] = u32x2{cvtpk(p[0], p[1]), cvtpk(p[2], p[3])};
#pragma unroll
            for (int d = 0; d < 8; ++d) acc[x][d] *= al;
          }
        }
        if (rem <= NEAR) {
          lds_vst<unsigned char>(lds, a_fl + 8, (unsigned char)one);
          if (rem == 1) {
#pragma unroll
            for (int x = 0; x < 2; ++x) lds_vst<f32x2>(lds, a_mlw + x * 512, f32x2{m[x], xg_sum(l[x])});
            lds_vst<unsigned char>(lds, a_fl, (unsigned char)one);
            asm volatile("s_waitcnt vmcnt(0)" ::: "memory");
          }
          if (rem == 2) asm volatile("s_waitcnt vmcnt(8)" ::: "memory");
        }
        asm volatile("s_waitcnt vmcnt(%0)" :: "i"(NS * 8 - 4) : "memory");
        s16x4 vt[8];
#pragma unroll
        for (int d = 0; d < 8; ++d) {
          typedef __attribute__((ext_vector_type(4))) short v4i16;
          vt[d] = __builtin_amdgcn_ds_read_tr16_b64_v4i16((__attribute__((address_space(3))) v4i16*)(lds + (va[d] + u * 8192)));
        }
        asm volatile("s_waitcnt lgkmcnt(0)" : "+v"(vt[0]), "+v"(vt[1]), "+v"(vt[2]), "+v"(vt[3]), "+v"(vt[4]), "+v"(vt[5]), "+v"(vt[6]), "+v"(vt[7]) :: "memory");
        if (rem > NS) dma4(vvo, rv, so, ring + u * 8192 + 4096);
#pragma unroll
        for (int x = 0; x < 2; ++x) {
          if (jb & (COMPM << (4 * x))) {
#pragma unroll
            for (int d = 0; d < 8; ++d)
              acc[x][d] = __builtin_amdgcn_mfma_f32_16x16x16bf16_1k(vt[d], __builtin_bit_cast(s16x4, pb[x]), acc[x][d], 0, 0, 0);
          }
        }
      }
    }
    STAMP(4);
#pragma unroll
    for (int x = 0; x < 2; ++x) {
      if ((wgmask | ownmask) & (0xF << (4 * x))) {
#pragma unroll
        for (int d = 0; d < 8; ++d) lds_vst<f32x4>(lds, a_rec + x * 16 * ROW + d * 64, acc[x][d]);
      }
    }
    if (n <= 0) {
#pragma unroll
      for (int x = 0; x < 2; ++x) lds_vst<f32x2>(lds, a_mlw + x * 512, f32x2{-FLT_MAX, 0.f});
      lds_vst<unsigned char>(lds, a_fl + 8, (unsigned char)one);
      lds_vst<unsigned char>(lds, a_fl, (unsigned char)one);
    }
    lds_vst<unsigned char>(lds, a_fl + 4, (unsigned char)one);
  }
  STAMP(6);
  STAMP_OUT();
}
}

extern "C" void kernel_launch(void* const* d_in, const int* in_sizes, int n_in, void* d_out, int out_size, void* d_ws, size_t ws_size,
                              hipStream_t stream) {
  attn_decode<<<256, NTHR, 0, stream>>>((const unsigned char*)d_in[0], (const unsigned char*)d_in[1], (const unsigned char*)d_in[2],
                                              (const int*)d_in[3], (const int*)d_in[4], (unsigned char*)d_ws, (unsigned short*)d_out,
                                              (long long)(in_sizes[1] / (PAGE * HKV * D)));
}
```

```cpp
#include <hip/hip_runtime.h>
#include <hip/hip_bf16.h>
#include <stdint.h>
#include <float.h>

#define SHR_A 8
#define OWN_B 0
#define DESC 4
#define NEAR 2
#define LAZY 8
#define NBT 6
#define HSTART 0, 29, 69, 98, 127, 156, 196, 225, 256
#define NS 2
#define COMPM 0xF0000

namespace {
constexpr int B = 8, H = 32, HKV = 8, G = 4, D = 128, PAGE = 16, NB = 512, LK = NB * PAGE;
constexpr int HS[9] = {HSTART};
constexpr unsigned long long pack8(int cnt) { unsigned long long r = 0; for (int h = 0; h < 8; ++h) r |= (unsigned long long)(cnt ? HS[h + 1] - HS[h] : HS[h]) << (8 * h); return r; }
constexpr unsigned long long packr(int h0) { unsigned long long r = 0; for (int h = 0; h < 4; ++h) r |= (unsigned long long)((65535 + HS[h0 + h + 1] - HS[h0 + h]) / (HS[h0 + h + 1] - HS[h0 + h])) << (16 * h); return r; }
constexpr int NWAVE = 8, NTHR = NWAVE * 64, NSTR = 4;
constexpr int PAGE_BYTES = PAGE * HKV * D * 2;
constexpr float CSC = 0.088388347648318440f * 1.4426950408889634f;
constexpr int ROW = 528, RECW = 32 * ROW;
constexpr int L_TBL = 0;
constexpr int L_Q = L_TBL + B * NB * 4;
constexpr int L_ML = L_Q + 32 * 256;
constexpr int L_FM = L_ML + 32 * 32;
constexpr int L_CNT = L_FM + 32;
constexpr int L_RING = 32768 - 4096;
constexpr int RINGW = NS * 8192;
constexpr int L_REC = L_RING + NSTR * RINGW;
constexpr int RECS = RECW;
constexpr int L_END = L_REC + NSTR * RECS;
static_assert(L_CNT + 16 <= L_RING && L_END <= 163840, "");
constexpr int WS_EPOCH = 0;
constexpr int AREA = B * 4096 + 1024;
constexpr int WS_PART = 4096;
constexpr int WS_BYTES = WS_PART + 256 * AREA;
constexpr unsigned long long TAG_STEP = 0x9E3779B97F4A7C15ull;

typedef __attribute__((ext_vector_type(4))) unsigned u32x4;
typedef __attribute__((ext_vector_type(2))) unsigned u32x2;
typedef __attribute__((ext_vector_type(4))) float f32x4;
typedef __attribute__((ext_vector_type(2))) float f32x2;
typedef __attribute__((ext_vector_type(8))) __bf16 bf16x8;
typedef __attribute__((ext_vector_type(4))) short s16x4;
typedef __attribute__((address_space(3))) unsigned char* ldsp;
typedef __attribute__((address_space(1))) unsigned long long* gu64p;

__device__ __forceinline__ unsigned cvtpk(float lo, float hi) {
  typedef __attribute__((ext_vector_type(2))) __bf16 bf16x2;
  bf16x2 r = {(__bf16)lo, (__bf16)hi};
  return __builtin_bit_cast(unsigned, r);
}
__device__ __forceinline__ float xg_max(float x) {
  auto s = __builtin_amdgcn_permlane16_swap(__float_as_uint(x), __float_as_uint(x), false, false);
  x = fmaxf(__uint_as_float(s[0]), __uint_as_float(s[1]));
  auto t = __builtin_amdgcn_permlane32_swap(__float_as_uint(x), __float_as_uint(x), false, false);
  return fmaxf(__uint_as_float(t[0]), __uint_as_float(t[1]));
}
__device__ __forceinline__ float xg_sum(float x) {
  auto s = __builtin_amdgcn_permlane16_swap(__float_as_uint(x), __float_as_uint(x), false, false);
  x = __uint_as_float(s[0]) + __uint_as_float(s[1]);
  auto t = __builtin_amdgcn_permlane32_swap(__float_as_uint(x), __float_as_uint(x), false, false);
  return __uint_as_float(t[0]) + __uint_as_float(t[1]);
}
template <int SH> __device__ __forceinline__ int row_shr(int x) { return __builtin_amdgcn_update_dpp(0, x, 0x110 + SH, 0xF, 0xF, true); }
template <int CTL> __device__ __forceinline__ float dpp(float x) { return __int_as_float(__builtin_amdgcn_update_dpp(0, __float_as_int(x), CTL, 0xF, 0xF, true)); }
#define DPP2(op, a, b) "s_nop 1\n\t" op " %0, %0, %0 " a " row_mask:0xf bank_mask:0xf\n\ts_nop 1\n\t" op " %0, %0, %0 " b " row_mask:0xf bank_mask:0xf\n\t"
__device__ __forceinline__ float quad_max(float x) { asm(DPP2("v_max_f32_dpp", "quad_perm:[1,0,3,2]", "quad_perm:[2,3,0,1]") : "+v"(x)); return x; }
__device__ __forceinline__ float quad_sum(float x) { asm(DPP2("v_add_f32_dpp", "quad_perm:[1,0,3,2]", "quad_perm:[2,3,0,1]") : "+v"(x)); return x; }
__device__ __forceinline__ float row_max(float x) { asm(DPP2("v_max_f32_dpp", "row_ror:8", "row_ror:4") DPP2("v_max_f32_dpp", "row_ror:2", "row_ror:1") : "+v"(x)); return x; }
__device__ __forceinline__ float row_sum(float x) { asm(DPP2("v_add_f32_dpp", "row_ror:8", "row_ror:4") DPP2("v_add_f32_dpp", "row_ror:2", "row_ror:1") : "+v"(x)); return x; }
__device__ __forceinline__ void rec_reads(u32x2& fl, f32x2& ml, f32x2 (&xv)[NSTR], int a_cnt, int a_ml, int a_x) {
  asm volatile("ds_read_b64 %0, %6\n\tds_read_b64 %1, %7\n\tds_read_b64 %2, %8\n\tds_read_b64 %3, %8 offset:%9\n\tds_read_b64 %4, %8 offset:%10\n\tds_read_b64 %5, %8 offset:%11\n\ts_waitcnt lgkmcnt(0)"
               : "=&v"(fl), "=&v"(ml), "=&v"(xv[0]), "=&v"(xv[1]), "=&v"(xv[2]), "=&v"(xv[3]) : "v"(a_cnt), "v"(a_ml), "v"(a_x), "i"(RECS), "i"(2 * RECS), "i"(3 * RECS) : "memory");
}
constexpr int ALL_DONE = 0x01010101;
__device__ __forceinline__ float rdl(float x, int l) { return __int_as_float(__builtin_amdgcn_readlane(__float_as_int(x), l)); }
template <typename T> __device__ __forceinline__ T lds_ld(ldsp p, int off) { return *(__attribute__((address_space(3))) T*)(p + off); }
template <typename T> __device__ __forceinline__ T lds_vld(ldsp p, int off) { return *(volatile __attribute__((address_space(3))) T*)(p + off); }
template <typename T> __device__ __forceinline__ void lds_st(ldsp p, int off, T v) { *(__attribute__((address_space(3))) T*)(p + off) = v; }
template <typename T> __device__ __forceinline__ void lds_vst(ldsp p, int off, T v) { *(volatile __attribute__((address_space(3))) T*)(p + off) = v; }
__device__ __forceinline__ u32x4 seal(f32x2 v, unsigned tl, unsigned th) {
  return u32x4{__float_as_uint(v[0]), __float_as_uint(v[1]), __float_as_uint(v[0]) ^ tl, __float_as_uint(v[1]) ^ th};
}
__device__ __forceinline__ unsigned stale(u32x4 g, unsigned tl, unsigned th) { return (g[0] ^ g[2] ^ tl) | (g[1] ^ g[3] ^ th); }
__device__ __forceinline__ void dma4(const int (&vo)[4], u32x4 rs, unsigned soff, unsigned ldsaddr) {
  asm volatile("s_mov_b32 m0, %6\n\ts_nop 4\n\t"
               "buffer_load_dwordx4 %0, %4, %5 offen nt lds\n\t"
               "buffer_load_dwordx4 %1, %4, %5 offen offset:1024 nt lds\n\t"
               "buffer_load_dwordx4 %2, %4, %5 offen offset:2048 nt lds\n\t"
               "buffer_load_dwordx4 %3, %4, %5 offen offset:3072 nt lds"
               :: "v"(vo[0]), "v"(vo[1]), "v"(vo[2]), "v"(vo[3]), "s"(rs), "s"(soff), "s"(ldsaddr) : "memory");
}

__global__ void __launch_bounds__(NTHR, 2)
attn_decode(const unsigned char* __restrict__ q, const unsigned char* kc, const unsigned char* vc,
            const int* __restrict__ bt, const int* __restrict__ ctxl, unsigned char* ws, unsigned short* out, long long nphys) {
  __shared__ __attribute__((aligned(16))) unsigned char smem[L_END];
  const ldsp lds = (ldsp)smem;
  const int tid = threadIdx.x, lane = tid & 63;
  const int w = __builtin_amdgcn_readfirstlane(tid >> 6);
  const int g = lane >> 4, c = lane & 15;
  int kvh = 0;
#pragma unroll
  for (int h = 1; h < HKV; ++h) kvh += blockIdx.x >= HS[h];
  const int hs = (int)(pack8(0) >> (8 * kvh)) & 255, nw = (int)(pack8(1) >> (8 * kvh)) & 255;
  int wi = blockIdx.x - hs, rnw = (int)((kvh < 4 ? packr(0) : packr(4)) >> (16 * (kvh & 3))) & 0xFFFF;
  asm volatile("" : "+s"(kc), "+s"(vc), "+s"(out), "+s"(nphys), "+s"(wi), "+s"(rnw));
  if (w < NSTR) __builtin_amdgcn_s_setprio(3);
  int cxv = ctxl[lane & 7];
  int limv[2] = {ctxl[c >> 2], ctxl[4 + (c >> 2)]};
  int e[B];
#pragma unroll
  for (int b = 0; b < B; ++b) e[b] = bt[b * NB + tid];
  const u32x4 qv = *reinterpret_cast<const u32x4*>(q + (w * H + kvh * G) * D * 2 + lane * 16);
  unsigned long long tag = __hip_atomic_load((gu64p)(ws + WS_EPOCH) + kvh * B + ((lane >> 2) & 7), __ATOMIC_RELAXED, __HIP_MEMORY_SCOPE_AGENT);
  int kvo[4], vvo[4];
#pragma unroll
  for (int t = 0; t < 4; ++t) {
    const int row = 4 * t + g;
    kvo[t] = row * 2048 + ((c ^ row) * 16) + kvh * 256 - t * 1024;
    vvo[t] = row * 2048 + ((c ^ ((row & 7) << 1)) * 16) + kvh * 256 - t * 1024;
  }
  int fw = w + NSTR * lane;
  asm volatile("" : "+v"(kvo[0]), "+v"(kvo[1]), "+v"(kvo[2]), "+v"(kvo[3]), "+v"(vvo[0]), "+v"(vvo[1]), "+v"(vvo[2]), "+v"(vvo[3]), "+v"(fw));
  cxv = min(max(cxv, 0), LK);
#pragma unroll
  for (int x = 0; x < 2; ++x) limv[x] = min(max(limv[x], 0), LK) - 4 * g;
  asm volatile("" : "+v"(limv[0]), "+v"(limv[1]));
  int npv = (cxv + PAGE - 1) / PAGE;
  int npmax;
  {
    int t = max(npv, row_shr<1>(npv)); t = max(t, row_shr<2>(t)); t = max(t, row_shr<4>(t));
    npmax = __builtin_amdgcn_readlane(t, 7);
  }
  bool mis = false;
#pragma unroll
  for (int b = 0; b < B; ++b) { lds_st<int>(lds, L_TBL + (b * NB + tid) * 4, e[b]); mis |= (e[b] != e[0]); }
  {
    const unsigned long long bal = __ballot(mis);
    int first = min(bal ? (w * 64 + __builtin_ctzll(bal)) : NB, npmax);
    first = npmax ? first : 1;
    if (lane < 4) lds_st<int>(lds, lane ? L_CNT - 4 + lane * 4 : L_FM + w * 4, lane ? 0 : first);
  }
  lds_st<u32x4>(lds, L_Q + (4 * w + g) * 256 + ((c ^ ((4 * w + g) & 15)) * 16), qv);
  tag += TAG_STEP;
  unsigned tl = (unsigned)tag, th = (unsigned)(tag >> 32);
  asm volatile("" : "+v"(tl), "+v"(th));
  __syncthreads();
  int S;
  {
    const u32x4 f0 = lds_ld<u32x4>(lds, L_FM), f1 = lds_ld<u32x4>(lds, L_FM + 16);
    const unsigned s = min(min(min(f0[0], f0[1]), min(f0[2], f0[3])), min(min(f1[0], f1[1]), min(f1[2], f1[3])));
    S = __builtin_amdgcn_readfirstlane((int)s);
  }
  const int cnt = max(npv - S, 0);
  int se = cnt + row_shr<1>(cnt); se += row_shr<2>(se); se += row_shr<4>(se); se += S;
  const int sb = se - cnt;
  const int Wp = __builtin_amdgcn_readlane(se, 7);
  const int beta = (Wp * OWN_B) >> 12, ash = S * (SHR_A << 8);
  const int wr = ((((Wp - B * beta) << 6) + (ash >> 10)) * rnw) >> 6;
  const int mid = cnt ? sb + (cnt >> 1) : ((2 * (lane & 7) + 1) * Wp) >> 4;
  const int ymid = max(((mid - (lane & 7) * beta) << 16) + ash, 0);
  const int y0 = wi * wr, y1 = wi + 1 == nw ? 0x7FFFFFFF : y0 + wr;
  const int m0 = (int)(__ballot(ymid < y0) & 0xFF), m1 = (int)(__ballot(ymid < y1) & 0xFF);
  const int c0 = min(((y0 - min((y0 >> 8) * SHR_A, ash)) >> 16) + beta * __builtin_popcount(m0), Wp);
  const int c1 = min(((y1 - min((y1 >> 8) * SHR_A, ash)) >> 16) + beta * __builtin_popcount(m1), Wp);
  const int ownmask = m1 & ~m0;
  const int priv = (int)(__ballot(max(sb, c0) < min(se, c1)) & 0xFF);
  const int wgmask = (c0 < S && c0 < c1) ? 0xFF : priv;
  const auto rw = __builtin_amdgcn_make_buffer_rsrc(ws, 0, WS_BYTES, 0x00020000);
  const int n = (c1 - c0 - w + NSTR - 1) >> 2;
  if (__builtin_expect(w >= NSTR, 0)) {
    const int r = w - NSTR, lb = (int)(uintptr_t)lds, a_cnt = lb + L_CNT;
    const int pub = wgmask & ~ownmask;
    const int vo = r * 1024 + lane * 16;
    const int yj = lane * wr, yn = lane + 1 >= nw ? 0x7FFFFFFF : yj + wr;
    int cj = (yj - min((yj >> 8) * SHR_A, ash)) >> 16, cn = (yn - min((yn >> 8) * SHR_A, ash)) >> 16;
#pragma unroll
    for (int b = 0; b < B; ++b) { const int ym = __builtin_amdgcn_readlane(ymid, b); cj += ym < yj ? beta : 0; cn += ym < yn ? beta : 0; }
    cj = min(cj, Wp); cn = min(cn, Wp);
    const int hb = WS_PART + hs * AREA, mine = hb + wi * AREA;
    int spin = 0;
    if (pub) {
      const int b0 = __builtin_ctz(pub);
      int a_ml = lb + L_ML + (((lane >> 2) & 7) * 4 + r) * 32 + (lane & 3) * 8, a_rd = L_REC + r * ROW + lane * 8, a_x = lb + a_rd + b0 * 4 * ROW;
      int o_ml = (lane < 32 && (lane & 3) == 0 && ((pub >> (lane >> 2)) & 1)) ? mine + B * 4096 + (lane + r) * 16 : WS_BYTES;
      unsigned tl0 = __builtin_amdgcn_readlane(tl, 4 * b0), th0 = __builtin_amdgcn_readlane(th, 4 * b0);
      int so0 = mine + b0 * 4096;
      asm volatile("" : "+v"(a_ml), "+v"(a_rd), "+v"(a_x), "+v"(o_ml), "+s"(tl0), "+s"(th0), "+s"(so0));
      while (__builtin_amdgcn_readfirstlane(lds_vld<int>(lds, L_CNT + 8)) != ALL_DONE) __builtin_amdgcn_s_sleep(4);
      __builtin_amdgcn_raw_buffer_store_b128(seal(f32x2{0.f, -1.f}, tl, th), rw, o_ml, 0, 16);
      u32x2 fl; f32x2 ml, xv[NSTR];
#pragma unroll 1
      for (; spin < (1 << 20); ++spin) {
        rec_reads(fl, ml, xv, a_cnt, a_ml, a_x);
        if (__builtin_amdgcn_readfirstlane(fl[0]) == ALL_DONE) break;
        __builtin_amdgcn_s_sleep(1);
      }
      const float e = __builtin_floorf(quad_max(ml[0]));
      const float wt = __builtin_amdgcn_exp2f(ml[0] - e);
      __builtin_amdgcn_raw_buffer_store_b128(seal(f32x2{e, quad_sum(wt * ml[1])}, tl, th), rw, o_ml, 0, 16);
      float w0 = rdl(wt, 4 * b0), w1 = rdl(wt, 4 * b0 + 1), w2 = rdl(wt, 4 * b0 + 2), w3 = rdl(wt, 4 * b0 + 3);
      asm volatile("" : "+s"(w0), "+s"(w1), "+s"(w2), "+s"(w3));
#pragma unroll 1
      for (; __builtin_amdgcn_readfirstlane(fl[1]) != ALL_DONE && spin < (1 << 21); ++spin) rec_reads(fl, ml, xv, a_cnt, a_ml, a_x);
      __builtin_amdgcn_raw_buffer_store_b128(seal(w0 * xv[0] + w1 * xv[1] + w2 * xv[2] + w3 * xv[3], tl0, th0), rw, vo, so0, 16);
      if (pub == 0xFF) {
        f32x2 xa[B][NSTR];
#pragma unroll
        for (int b = 1; b < B; ++b)
#pragma unroll
          for (int k = 0; k < NSTR; ++k) xa[b][k] = lds_vld<f32x2>(lds, a_rd + b * 4 * ROW + k * RECS);
#pragma unroll
        for (int b = 1; b < B; ++b) {
          f32x2 o = rdl(wt, 4 * b) * xa[b][0];
#pragma unroll
          for (int k = 1; k < NSTR; ++k) o += rdl(wt, 4 * b + k) * xa[b][k];
          __builtin_amdgcn_raw_buffer_store_b128(seal(o, __builtin_amdgcn_readlane(tl, 4 * b), __builtin_amdgcn_readlane(th, 4 * b)), rw, vo, mine + b * 4096, 16);
        }
      } else {
        for (int pm = pub & (pub - 1); pm; pm &= pm - 1) {
          const int b = __builtin_ctz(pm);
          f32x2 o = rdl(wt, 4 * b) * lds_vld<f32x2>(lds, a_rd + b * 4 * ROW);
#pragma unroll
          for (int k = 1; k < NSTR; ++k) o += rdl(wt, 4 * b + k) * lds_vld<f32x2>(lds, a_rd + b * 4 * ROW + k * RECS);
          __builtin_amdgcn_raw_buffer_store_b128(seal(o, __builtin_amdgcn_readlane(tl, 4 * b), __builtin_amdgcn_readlane(th, 4 * b)), rw, vo, mine + b * 4096, 16);
        }
      }
    }
    for (int om = ownmask; om; om &= om - 1) {
      const int b = __builtin_ctz(om), row = b * 4 + r;
      const unsigned tlb = __builtin_amdgcn_readlane(tl, 4 * b), thb = __builtin_amdgcn_readlane(th, 4 * b);
      const bool con = (cj < S && cj < cn) || max(cj, __builtin_amdgcn_readlane(sb, b)) < min(cn, __builtin_amdgcn_readlane(se, b));
      int vml = (con && lane < nw && lane != wi) ? hb + lane * AREA + B * 4096 + row * 16 : WS_BYTES;
      unsigned long long pa = __ballot(vml != WS_BYTES), pw = pa, pb = 0, pn = 0;
      const int sd = hb + b * 4096, cx = __builtin_amdgcn_readlane(cxv, b);
      int a_ml = lb + L_ML + row * 32 + (lane & 3) * 8, a_x = lb + L_REC + row * ROW + lane * 8;
      unsigned short* op = out + (b * H + kvh * G + r) * D + lane * 2;
      float ej = -FLT_MAX, lj = 0.f, E = -FLT_MAX, Lx = 0.f, sc = 0.f, w0 = 0.f, w1 = 0.f, w2 = 0.f, w3 = 0.f;
      int wh = 0;
      double s0 = 0., s1 = 0.;
      f32x2 o = {0.f, 0.f};
      int st = 0;
      asm volatile("" : "+v"(vml), "+v"(a_ml), "+v"(a_x), "+v"(op));
      while (__builtin_amdgcn_readfirstlane(lds_vld<int>(lds, L_CNT + 8)) == 0) __builtin_amdgcn_s_sleep(8);
#pragma unroll 1
      for (; spin < (1 << 21); ++spin) {
        u32x4 gm, gx[NBT];
        int kk[NBT];
        unsigned long long t = pb;
#pragma unroll
        for (int j = 0; j < NBT; ++j) {
          if (!t) break;
          kk[j] = __builtin_ctzll(t); t &= t - 1;
          gx[j] = __builtin_amdgcn_raw_buffer_load_b128(rw, vo, sd + kk[j] * AREA, 16);
        }
        if (pa) gm = __builtin_amdgcn_raw_buffer_load_b128(rw, vml, 0, 16);
        u32x2 fl; f32x2 ml, xv[NSTR];
        rec_reads(fl, ml, xv, a_cnt, a_ml, a_x);
        bool idle = false;
        if (pa) {
          const bool ok = vml != WS_BYTES && stale(gm, tlb, thb) == 0, fin = ok && (int)gm[1] >= 0;
          const unsigned long long nv = __ballot(ok) & pw, nf = __ballot(fin);
          pw &= ~nv; pn = nv;
          idle = !pb && !nv;
          if (nf) {
            ej = fin ? __uint_as_float(gm[0]) : ej; lj = fin ? __uint_as_float(gm[1]) : lj;
            vml = fin ? WS_BYTES : vml;
            pa &= ~nf;
            const float En = xg_max(row_max(ej));
            const int dj = (int)fmaxf(ej - En, -1023.f), dE = (int)fmaxf(E - En, -1023.f);
            wh = (dj + 1023) << 20;
            Lx = xg_sum(row_sum(__builtin_amdgcn_ldexpf(lj, dj)));
            s0 = __builtin_amdgcn_ldexp(s0, dE); s1 = __builtin_amdgcn_ldexp(s1, dE);
            E = En;
          }
        }
        if (!(st & 2) && !pa && __builtin_amdgcn_readfirstlane(fl[0]) == ALL_DONE) {
          float M = fmaxf(E, quad_max(ml[0])), L = 0.f;
          constexpr float MASKED = -998244352.0f * 1.4426950408889634f;
          if (__builtin_expect(cx < LK && MASKED - M > -200.f, 0)) {
            for (int p = cx; p < LK; ++p) {
              int ph = bt[b * NB + (p >> 4)];
              ph = ph < 0 ? ph + (int)nphys : ph;
              const unsigned vv = *(const __attribute__((address_space(1))) unsigned*)(vc + (size_t)ph * PAGE_BYTES + (p & 15) * 2048 + kvh * 256 + lane * 4);
              o += f32x2{__uint_as_float(vv << 16), __uint_as_float(vv & 0xFFFF0000u)};
            }
            M = fmaxf(M, MASKED);
            const float wm = __builtin_amdgcn_exp2f(MASKED - M);
            L = wm * (float)(LK - cx);
            o *= wm;
          }
          const float a = __builtin_amdgcn_exp2f(E - M);
          float wt = __builtin_amdgcn_exp2f(ml[0] - M);
          const float inv = __builtin_amdgcn_rcpf(L + __builtin_fmaf(Lx, a, quad_sum(wt * ml[1])));
          sc = a * inv; wt *= inv; o *= inv;
          w0 = rdl(wt, 0); w1 = rdl(wt, 1); w2 = rdl(wt, 2); w3 = rdl(wt, 3);
          st |= 2;
        }
        if (st == 2 && __builtin_amdgcn_readfirstlane(fl[1]) == ALL_DONE) {
          o += w0 * xv[0]; o += w1 * xv[1]; o += w2 * xv[2]; o += w3 * xv[3];
          st = 6;
        }
        t = pb;
#pragma unroll
        for (int j = 0; j < NBT; ++j) {
          if (!t) break;
          t &= t - 1;
          if (!__ballot(stale(gx[j], tlb, thb) != 0) && !((pa >> kk[j]) & 1)) {
            const double wk = __hiloint2double(__builtin_amdgcn_readlane(wh, kk[j]), 0);
            s0 = __builtin_fma((double)__uint_as_float(gx[j][0]), wk, s0);
            s1 = __builtin_fma((double)__uint_as_float(gx[j][1]), wk, s1);
            pb &= ~(1ull << kk[j]);
          }
        }
        pb |= pn; pn = 0;
        if (!pb && st == 6) break;
        if (idle && __builtin_amdgcn_readfirstlane(fl[1]) != ALL_DONE) __builtin_amdgcn_s_sleep(LAZY);
      }
      const unsigned res = cvtpk(__builtin_fmaf((float)s0, sc, o[0]), __builtin_fmaf((float)s1, sc, o[1]));
      asm volatile("global_store_dword %0, %1, off sc0 sc1" :: "v"(op), "v"(res) : "memory");
      if (tid == NSTR * 64) __hip_atomic_store((gu64p)(ws + WS_EPOCH) + kvh * B + b, ((unsigned long long)thb << 32) | tlb, __ATOMIC_RELAXED, __HIP_MEMORY_SCOPE_AGENT);
    }
    __builtin_amdgcn_s_waitcnt(0);
    __builtin_amdgcn_endpgm();
  }
  {
    unsigned dphys; int djb;
    {
      const int f = (wi & DESC) ? max(c1 - 1 - fw, 0) : min(c0 + fw, Wp - 1);
      const int dl = S + (lane & 7) * NB - sb;
      int idx = f;
      for (int pm = priv; pm; pm &= pm - 1) {
        const int b = __builtin_ctz(pm);
        const int s0 = __builtin_amdgcn_readlane(sb, b), d0 = __builtin_amdgcn_readlane(dl, b);
        idx = f >= s0 ? f + d0 : idx;
      }
      int ph = lds_ld<int>(lds, L_TBL + (idx & (B * NB - 1)) * 4);
      ph = ph < 0 ? ph + (int)nphys : ph;
      dphys = (unsigned)ph * PAGE_BYTES;
      djb = ((idx & (NB - 1)) * PAGE) | (f < S ? 0xFF0000 : 0x10000 << ((idx >> 9) & 7));
    }
    const unsigned cbytes = (unsigned)nphys * PAGE_BYTES;
    const u32x4 rk = {(unsigned)(uintptr_t)kc, (unsigned)((uintptr_t)kc >> 32), cbytes, 0x00020000u};
    const u32x4 rv = {(unsigned)(uintptr_t)vc, (unsigned)((uintptr_t)vc >> 32), cbytes, 0x00020000u};
    const unsigned ring = (unsigned)(uintptr_t)lds + L_RING + w * RINGW;
#pragma unroll
    for (int u = 0; u < NS; ++u) {
      if (u >= n) break;
      const unsigned so = __builtin_amdgcn_readlane(dphys, u);
      dma4(kvo, rk, so, ring + u * 8192);
      dma4(vvo, rv, so, ring + u * 8192 + 4096);
    }
    u32x4 qf[2][4];
#pragma unroll
    for (int x = 0; x < 2; ++x)
#pragma unroll
      for (int kk = 0; kk < 4; ++kk) qf[x][kk] = lds_ld<u32x4>(lds, L_Q + (x * 16 + c) * 256 + (((4 * kk + g) ^ c) * 16));
    int bbit[2];
#pragma unroll
    for (int x = 0; x < 2; ++x) bbit[x] = 1 << (16 + 4 * x + (c >> 2));
    int ka[4], va[8];
#pragma unroll
    for (int kk = 0; kk < 4; ++kk) ka[kk] = L_RING + w * RINGW + c * 256 + (((4 * kk + g) ^ c) * 16);
#pragma unroll
    for (int d = 0; d < 8; ++d) va[d] = L_RING + w * RINGW + 4096 + (4 * g + (c >> 2)) * 256 + ((((4 * g + (c >> 2)) & 7) ^ d) << 5) + ((c & 3) << 3);
    int a_rec = L_REC + w * RECS + c * ROW + g * 16, a_mlw = L_ML + c * 32 + w * 8, a_fl = L_CNT + w, one = 1;
    asm volatile("" : "+v"(a_rec), "+v"(a_mlw), "+v"(a_fl), "+v"(one));

    f32x4 acc[2][8];
    float m[2], l[2];
#pragma unroll
    for (int x = 0; x < 2; ++x) { m[x] = -FLT_MAX; l[x] = 0.f;
#pragma unroll
      for (int d = 0; d < 8; ++d) acc[x][d] = f32x4{0.f, 0.f, 0.f, 0.f}; }

    asm volatile("" : "+v"(qf[0][0]), "+v"(qf[0][1]), "+v"(qf[0][2]), "+v"(qf[0][3]), "+v"(qf[1][0]), "+v"(qf[1][1]), "+v"(qf[1][2]), "+v"(qf[1][3]));
    for (int i0 = 0; i0 < n; i0 += NS) {
#pragma unroll
      for (int u = 0; u < NS; ++u) {
        const int i = i0 + u;
        if (u && i >= n) break;
        const int jb = __builtin_amdgcn_readlane(djb, i);
        const unsigned so = __builtin_amdgcn_readlane(dphys, i + NS);
        const int rem = n - i;
        if (rem == 1) asm volatile("s_waitcnt vmcnt(4)" ::: "memory");
        asm volatile("s_waitcnt vmcnt(%0)" :: "i"(NS * 8 - 4) : "memory");
        u32x4 kf[4];
#pragma unroll
        for (int kk = 0; kk < 4; ++kk) kf[kk] = lds_ld<u32x4>(lds, ka[kk] + u * 8192);
        asm volatile("s_waitcnt lgkmcnt(0)" : "+v"(kf[0]), "+v"(kf[1]), "+v"(kf[2]), "+v"(kf[3]) :: "memory");
        if (rem > NS) dma4(kvo, rk, so, ring + u * 8192);
        u32x2 pb[2];
#pragma unroll
        for (int x = 0; x < 2; ++x) {
          if (jb & (COMPM << (4 * x))) {
            f32x4 s = {0.f, 0.f, 0.f, 0.f};
#pragma unroll
            for (int kk = 0; kk < 4; ++kk)
              s = __builtin_amdgcn_mfma_f32_16x16x32_bf16(__builtin_bit_cast(bf16x8, kf[kk]), __builtin_bit_cast(bf16x8, qf[x][kk]), s, 0, 0, 0);
            const int lim = (jb & bbit[x]) ? limv[x] - (jb & 0xFFFF) : 0;
#pragma unroll
            for (int r = 0; r < 4; ++r) s[r] = (r < lim) ? s[r] : -INFINITY;
            float mx = fmaxf(fmaxf(s[0], s[1]), fmaxf(s[2], s[3]));
            mx = xg_max(mx) * CSC;
            const float mn = fmaxf(m[x], mx);
            const float al = __builtin_amdgcn_exp2f(m[x] - mn);
            m[x] = mn;
            float p[4];
#pragma unroll
            for (int r = 0; r < 4; ++r) p[r] = __builtin_amdgcn_exp2f(__builtin_fmaf(s[r], CSC, -mn));
            l[x] = __builtin_fmaf(l[x], al, (p[0] + p[1]) + (p[2] + p[3]));
            pb[x] = u32x2{cvtpk(p[0], p[1]), cvtpk(p[2], p[3])};
#pragma unroll
            for (int d = 0; d < 8; ++d) acc[x][d] *= al;
          }
        }
        if (rem <= NEAR) {
          lds_vst<unsigned char>(lds, a_fl + 8, (unsigned char)one);
          if (rem == 1) {
#pragma unroll
            for (int x = 0; x < 2; ++x) lds_vst<f32x2>(lds, a_mlw + x * 512, f32x2{m[x], xg_sum(l[x])});
            lds_vst<unsigned char>(lds, a_fl, (unsigned char)one);
            asm volatile("s_waitcnt vmcnt(0)" ::: "memory");
          }
          if (rem == 2) asm volatile("s_waitcnt vmcnt(8)" ::: "memory");
        }
        asm volatile("s_waitcnt vmcnt(%0)" :: "i"(NS * 8 - 4) : "memory");
        s16x4 vt[8];
#pragma unroll
        for (int d = 0; d < 8; ++d) {
          typedef __attribute__((ext_vector_type(4))) short v4i16;
          vt[d] = __builtin_amdgcn_ds_read_tr16_b64_v4i16((__attribute__((address_space(3))) v4i16*)(lds + (va[d] + u * 8192)));
        }
        asm volatile("s_waitcnt lgkmcnt(0)" : "+v"(vt[0]), "+v"(vt[1]), "+v"(vt[2]), "+v"(vt[3]), "+v"(vt[4]), "+v"(vt[5]), "+v"(vt[6]), "+v"(vt[7]) :: "memory");
        if (rem > NS) dma4(vvo, rv, so, ring + u * 8192 + 4096);
#pragma unroll
        for (int x = 0; x < 2; ++x) {
          if (jb & (COMPM << (4 * x))) {
#pragma unroll
            for (int d = 0; d < 8; ++d)
              acc[x][d] = __builtin_amdgcn_mfma_f32_16x16x16bf16_1k(vt[d], __builtin_bit_cast(s16x4, pb[x]), acc[x][d], 0, 0, 0);
          }
        }
      }
    }
#pragma unroll
    for (int x = 0; x < 2; ++x) {
      if ((wgmask | ownmask) & (0xF << (4 * x))) {
#pragma unroll
        for (int d = 0; d < 8; ++d) lds_vst<f32x4>(lds, a_rec + x * 16 * ROW + d * 64, acc[x][d]);
      }
    }
    if (n <= 0) {
#pragma unroll
      for (int x = 0; x < 2; ++x) lds_vst<f32x2>(lds, a_mlw + x * 512, f32x2{-FLT_MAX, 0.f});
      lds_vst<unsigned char>(lds, a_fl + 8, (unsigned char)one);
      lds_vst<unsigned char>(lds, a_fl, (unsigned char)one);
    }
    lds_vst<unsigned char>(lds, a_fl + 4, (unsigned char)one);
  }
}
}

extern "C" void kernel_launch(void* const* d_in, const int* in_sizes, int n_in, void* d_out, int out_size, void* d_ws, size_t ws_size,
                              hipStream_t stream) {
  attn_decode<<<256, NTHR, 0, stream>>>((const unsigned char*)d_in[0], (const unsigned char*)d_in[1], (const unsigned char*)d_in[2],
                                              (const int*)d_in[3], (const int*)d_in[4], (unsigned char*)d_ws, (unsigned short*)d_out,
                                              (long long)(in_sizes[1] / (PAGE * HKV * D)));
}
```

```cpp
#include <hip/hip_runtime.h>
#include <hip/hip_bf16.h>
#include <stdint.h>
#include <float.h>

#define DESC 4
#define NEAR 2
#define EXTRA 9
#define EXS 7
#define CODEB 9216
#define HSTART 0, 29, 69, 98, 127, 156, 196, 225, 256
#define XD 2
#define ROT 3
#define NEARO 3
#define SHR 24
#define APAD 0
#define NS 4
#define COMPM 0xF0000

namespace {
constexpr int B = 8, H = 32, HKV = 8, G = 4, D = 128, PAGE = 16, NB = 512, LK = NB * PAGE;
constexpr int HS[9] = {HSTART};
constexpr unsigned long long pack8(int cnt) { unsigned long long r = 0; for (int h = 0; h < 8; ++h) r |= (unsigned long long)(cnt ? HS[h + 1] - HS[h] : HS[h]) << (8 * h); return r; }
constexpr unsigned long long packr(int h0, int sub) { unsigned long long r = 0; for (int h = 0; h < 4; ++h) r |= (unsigned long long)((65535 + HS[h0 + h + 1] - HS[h0 + h] - sub) / (HS[h0 + h + 1] - HS[h0 + h] - sub)) << (16 * h); return r; }
constexpr int NWAVE = 8, NTHR = NWAVE * 64, NSTR = 4;
constexpr int PAGE_BYTES = PAGE * HKV * D * 2;
constexpr float CSC = 0.088388347648318440f * 1.4426950408889634f;
constexpr int L_Q = 0;
constexpr int L_ML = L_Q + 32 * 256;
constexpr int L_FM = L_ML + 32 * 32;
constexpr int L_CNT = L_FM + 32;
constexpr int L_TBL = 16384;
constexpr int L_RING = 32768;
constexpr int SLOT = 32768, VOFF = 16384, RECS = 4096;
constexpr int ROW = 528, SEQ = 16384 + 64;
constexpr int L_END = L_RING + NS * SLOT;
static_assert(L_CNT + 32 <= L_TBL && L_END <= 163840, "");
constexpr int WS_EPOCH = 0;
constexpr int A_ML = B * 2048, AREA = A_ML + 512 + APAD;
constexpr int WS_PART = 4096;
constexpr int WS_BYTES = WS_PART + 256 * AREA;
constexpr unsigned TAG_STEP = 0x9E3779B9u, NEG = 0xFF7FFFFFu;

typedef __attribute__((ext_vector_type(4))) unsigned u32x4;
typedef __attribute__((ext_vector_type(2))) unsigned u32x2;
typedef __attribute__((ext_vector_type(4))) float f32x4;
typedef __attribute__((ext_vector_type(2))) float f32x2;
typedef __attribute__((ext_vector_type(8))) __bf16 bf16x8;
typedef __attribute__((ext_vector_type(4))) short s16x4;
typedef __attribute__((address_space(3))) unsigned char* ldsp;
typedef __attribute__((address_space(1))) unsigned* gu32p;

__device__ __forceinline__ unsigned cvtpk(float lo, float hi) {
  typedef __attribute__((ext_vector_type(2))) __bf16 bf16x2;
  bf16x2 r = {(__bf16)lo, (__bf16)hi};
  return __builtin_bit_cast(unsigned, r);
}
__device__ __forceinline__ float xg_max(float x) {
  auto s = __builtin_amdgcn_permlane16_swap(__float_as_uint(x), __float_as_uint(x), false, false);
  x = fmaxf(__uint_as_float(s[0]), __uint_as_float(s[1]));
  auto t = __builtin_amdgcn_permlane32_swap(__float_as_uint(x), __float_as_uint(x), false, false);
  return fmaxf(__uint_as_float(t[0]), __uint_as_float(t[1]));
}
__device__ __forceinline__ float xg_sum(float x) {
  auto s = __builtin_amdgcn_permlane16_swap(__float_as_uint(x), __float_as_uint(x), false, false);
  x = __uint_as_float(s[0]) + __uint_as_float(s[1]);
  auto t = __builtin_amdgcn_permlane32_swap(__float_as_uint(x), __float_as_uint(x), false, false);
  return __uint_as_float(t[0]) + __uint_as_float(t[1]);
}
template <int SH> __device__ __forceinline__ int row_shr(int x) { return __builtin_amdgcn_update_dpp(0, x, 0x110 + SH, 0xF, 0xF, true); }
template <int CTL> __device__ __forceinline__ float dpp(float x) { return __int_as_float(__builtin_amdgcn_update_dpp(0, __float_as_int(x), CTL, 0xF, 0xF, true)); }
#define DPP2(op, a, b) "s_nop 1\n\t" op " %0, %0, %0 " a " row_mask:0xf bank_mask:0xf\n\ts_nop 1\n\t" op " %0, %0, %0 " b " row_mask:0xf bank_mask:0xf\n\t"
__device__ __forceinline__ float quad_max(float x) { asm(DPP2("v_max_f32_dpp", "quad_perm:[1,0,3,2]", "quad_perm:[2,3,0,1]") : "+v"(x)); return x; }
__device__ __forceinline__ float quad_sum(float x) { asm(DPP2("v_add_f32_dpp", "quad_perm:[1,0,3,2]", "quad_perm:[2,3,0,1]") : "+v"(x)); return x; }
__device__ __forceinline__ float row_max(float x) { asm(DPP2("v_max_f32_dpp", "row_ror:8", "row_ror:4") DPP2("v_max_f32_dpp", "row_ror:2", "row_ror:1") : "+v"(x)); return x; }
__device__ __forceinline__ float row_sum(float x) { asm(DPP2("v_add_f32_dpp", "row_ror:8", "row_ror:4") DPP2("v_add_f32_dpp", "row_ror:2", "row_ror:1") : "+v"(x)); return x; }
#define REC_RD(T, RD) \
__device__ __forceinline__ void rec_reads(u32x2& fl, f32x2& ml, T (&xv)[NSTR], int a_cnt, int a_ml, int a_x) { \
  asm volatile("ds_read_b64 %0, %6\n\tds_read_b64 %1, %7\n\t" RD " %2, %8\n\t" RD " %3, %8 offset:%9\n\t" RD " %4, %8 offset:%10\n\t" RD " %5, %8 offset:%11\n\ts_waitcnt lgkmcnt(0)" \
               : "=&v"(fl), "=&v"(ml), "=&v"(xv[0]), "=&v"(xv[1]), "=&v"(xv[2]), "=&v"(xv[3]) : "v"(a_cnt), "v"(a_ml), "v"(a_x), "i"(RECS), "i"(2 * RECS), "i"(3 * RECS) : "memory"); \
} \
__device__ __forceinline__ void rec_wait(unsigned f1, T (&xv)[NSTR], int a_cnt, int a_x) { \
  int t; \
  asm volatile("s_nop 0\n1:\n\tv_readfirstlane_b32 %5, %0\n\ts_cmp_eq_u32 %5, 0x01010101\n\ts_cbranch_scc1 2f\n\t" \
               "ds_read_b32 %0, %6 offset:4\n\t" RD " %1, %7\n\t" RD " %2, %7 offset:%8\n\t" RD " %3, %7 offset:%9\n\t" RD " %4, %7 offset:%10\n\t" \
               "s_waitcnt lgkmcnt(0)\n\ts_branch 1b\n2:" \
               : "+v"(f1), "+v"(xv[0]), "+v"(xv[1]), "+v"(xv[2]), "+v"(xv[3]), "=&s"(t) : "v"(a_cnt), "v"(a_x), "i"(RECS), "i"(2 * RECS), "i"(3 * RECS) : "scc", "memory"); \
}
REC_RD(f32x2, "ds_read_b64")
REC_RD(f32x4, "ds_read_b128")
__device__ __forceinline__ unsigned ff1(unsigned long long m) { unsigned r; asm("s_ff1_i32_b64 %0, %1" : "=s"(r) : "s"(m)); return r; }
constexpr int ALL_DONE = 0x01010101;
__device__ __forceinline__ float rdl(float x, int l) { return __int_as_float(__builtin_amdgcn_readlane(__float_as_int(x), l)); }
template <typename T> __device__ __forceinline__ T lds_ld(ldsp p, int off) { return *(__attribute__((address_space(3))) T*)(p + off); }
template <typename T> __device__ __forceinline__ T lds_vld(ldsp p, int off) { return *(volatile __attribute__((address_space(3))) T*)(p + off); }
template <typename T> __device__ __forceinline__ void lds_st(ldsp p, int off, T v) { *(__attribute__((address_space(3))) T*)(p + off) = v; }
template <typename T> __device__ __forceinline__ void lds_vst(ldsp p, int off, T v) { *(volatile __attribute__((address_space(3))) T*)(p + off) = v; }
template <typename T> __device__ __forceinline__ T gld(const __attribute__((address_space(1))) unsigned char* p) { return *(const __attribute__((address_space(1))) T*)p; }
__device__ __forceinline__ u32x4 seal(f32x4 v, unsigned tag) {
  u32x4 d;
#pragma unroll
  for (int i = 0; i < 4; ++i) d[i] = __builtin_amdgcn_perm(__float_as_uint(v[i]), tag, 0x07060500 + i);
  return d;
}
__device__ __forceinline__ float bperm(int a, float v) { return __int_as_float(__builtin_amdgcn_ds_bpermute(a, __float_as_int(v))); }
__device__ __forceinline__ void dma4(const int (&vo)[4], u32x4 rs, unsigned soff, unsigned ldsaddr) {
  asm volatile("s_mov_b32 m0, %6\n\ts_nop 4\n\t"
               "buffer_load_dwordx4 %0, %4, %5 offen nt lds\n\t"
               "buffer_load_dwordx4 %1, %4, %5 offen offset:1024 nt lds\n\t"
               "buffer_load_dwordx4 %2, %4, %5 offen offset:2048 nt lds\n\t"
               "buffer_load_dwordx4 %3, %4, %5 offen offset:3072 nt lds"
               :: "v"(vo[0]), "v"(vo[1]), "v"(vo[2]), "v"(vo[3]), "s"(rs), "s"(soff), "s"(ldsaddr) : "memory");
}

__global__ void __launch_bounds__(NTHR, 2)
attn_decode(const void*, unsigned, unsigned, const void*, unsigned, unsigned, const void*, const void*, const void*, void*, unsigned short* out) {
  typedef __attribute__((ext_vector_type(16))) unsigned u32x16;
  u32x16 ka;
  asm volatile("s_load_dwordx16 %0, %1, 0x0" : "=s"(ka) : "s"(__builtin_amdgcn_kernarg_segment_ptr()));
  int pf;
  {
    unsigned long long pc;
    asm volatile("s_getpc_b64 %0" : "=s"(pc));
    const int po = min((int)(threadIdx.x & 255) * 64, CODEB);
    if (threadIdx.x >= 256) asm volatile("global_load_dword %0, %1, %2" : "=v"(pf) : "v"(po), "s"(pc));
  }
  __builtin_amdgcn_sched_barrier(0);
  __shared__ __attribute__((aligned(16))) unsigned char smem[L_END];
  const ldsp lds = (ldsp)smem;
  const int tid = threadIdx.x, lane = tid & 63;
  const int w = __builtin_amdgcn_readfirstlane(tid >> 6);
  const int g = lane >> 4, c = lane & 15;
  int kvh = 0;
#pragma unroll
  for (int h = 1; h < HKV; ++h) kvh += blockIdx.x >= HS[h];
  const int hs = (int)(pack8(0) >> (8 * kvh)) & 255, nw = (int)(pack8(1) >> (8 * kvh)) & 255;
  int wi = blockIdx.x - hs;
  int rnw = (int)((kvh < 4 ? packr(0, 0) : packr(4, 0)) >> (16 * (kvh & 3))) & 0xFFFF, rnc = (int)((kvh < 4 ? packr(0, B) : packr(4, B)) >> (16 * (kvh & 3))) & 0xFFFF;
  auto team = [&](int b) { return (nw >> 3) + (((b - ROT * (nw >> 3)) & 7) < (nw & 7)); };
  const int bh = (wi + ROT * (wi >> 3)) & 7;
  int hj = (lane * team(bh) + (wi >> 3)) * NSTR + w;
  int ho = (bh * NB + min(hj, NB - 1)) * 4, co = (lane & 7) * 4, lo = c & 12;
  int qo = ((w & 3) * 2 * H + kvh * G) * (D * 2) + lane * 16, qo2 = qo + H * D * 2;
  int t2 = (tid & 255) * 8, ta = t2 + 4096, tb = t2 + 12288, w2 = w, w3 = w;
  asm volatile("" : "+s"(w2), "+s"(w3));
  asm volatile("s_waitcnt lgkmcnt(0)" : "+s"(ka), "+s"(wi), "+s"(rnw), "+s"(rnc), "+v"(ho), "+v"(hj), "+v"(co), "+v"(lo), "+v"(qo), "+v"(qo2), "+v"(ta), "+v"(tb));
  typedef const __attribute__((address_space(1))) unsigned char* gcp;
  auto ptr = [&](int i) { return (gcp)(((unsigned long long)ka[i + 1] << 32) | ka[i]); };
  const gcp vc = ptr(4), q = ptr(8), bt = ptr(10), ctxl = ptr(12);
  unsigned char* const ws = (unsigned char*)ptr(14);
  const u32x4 rk = ka.s0123, rv = ka.s4567;
  const int nphys = ka[2] / PAGE_BYTES;
  int cxv, limv[2], j0;
  unsigned tag;
  const auto rw = __builtin_amdgcn_make_buffer_rsrc(ws, 0, WS_BYTES, 0x00020000);
  const gu32p epoch = (gu32p)(ws + WS_EPOCH) + (kvh * G + (w & 3)) * 16;
  int kvo[4], vvo[4];
  const unsigned ring = (unsigned)(uintptr_t)lds + L_RING + w * RECS;
  if (w < NSTR) {
    int hp;
    asm volatile("global_load_dword %0, %4, %7\n\tglobal_load_dword %1, %5, %8\n\tglobal_load_dword %2, %6, %8\n\tglobal_load_dword %3, %6, %8 offset:16"
                 : "=&v"(hp), "=&v"(cxv), "=&v"(limv[0]), "=&v"(limv[1]) : "v"(ho), "v"(co), "v"(lo), "s"(bt), "s"(ctxl));
    __builtin_amdgcn_sched_barrier(0);
    __builtin_amdgcn_s_setprio(3);
#pragma unroll
    for (int t = 0; t < 4; ++t) {
      const int row = 4 * t + g;
      kvo[t] = row * 2048 + ((c ^ row) * 16) + kvh * 256 - t * 1024;
      vvo[t] = row * 2048 + ((c ^ ((row & 7) << 1)) * 16) + kvh * 256 - t * 1024;
    }
    hj = hj < NB ? hj * PAGE : 0x7FFFFFFF;
    asm volatile("" : "+v"(kvo[0]), "+v"(kvo[1]), "+v"(kvo[2]), "+v"(kvo[3]), "+v"(vvo[0]), "+v"(vvo[1]), "+v"(vvo[2]), "+v"(vvo[3]), "+v"(hj));
    asm volatile("s_waitcnt vmcnt(2)" : "+v"(hp), "+v"(cxv));
    const int hl = 63 - __builtin_clzll(__ballot(hj < __builtin_amdgcn_readlane(cxv, bh)) | 1);
    int ph = __builtin_amdgcn_readlane(hp, hl);
    ph = ph < 0 ? ph + nphys : ph;
    dma4(kvo, rk, (unsigned)ph * PAGE_BYTES, ring);
    dma4(vvo, rv, (unsigned)ph * PAGE_BYTES, ring + VOFF);
    j0 = __builtin_amdgcn_readlane(hj, hl);
    asm volatile("s_waitcnt vmcnt(8)" : "+v"(limv[0]), "+v"(limv[1]));
  }
  if (w2 >= NSTR) {
    u32x2 e[B];
#pragma unroll
    for (int b = 0; b < B; ++b) e[b] = gld<u32x2>(bt + (unsigned)(b < 4 ? ta : tb) + ((b & 3) * NB * 4 - 4096));
    cxv = gld<int>(ctxl + (unsigned)co);
    const u32x4 qv[2] = {gld<u32x4>(q + (unsigned)qo), gld<u32x4>(q + (unsigned)qo2)};
    tag = __hip_atomic_load(epoch + ((lane >> 2) & 7), __ATOMIC_RELAXED, __HIP_MEMORY_SCOPE_AGENT);
    __builtin_amdgcn_sched_barrier(0);
    lds_st<int>(lds, L_CNT + co, 0);
    unsigned mis0 = 0, mis1 = 0;
#pragma unroll
    for (int b = 0; b < B; ++b) {
      lds_st<u32x2>(lds, L_TBL + b * NB * 4 + t2, e[b]);
      if (b) { mis0 = __builtin_amdgcn_bitop3_b32(e[b][0], e[0][0], mis0, 0xBE); mis1 = __builtin_amdgcn_bitop3_b32(e[b][1], e[0][1], mis1, 0xBE); }
    }
    lds_st<unsigned>(lds, L_FM - NSTR * 4 + w * 4, min(ff1(__ballot(mis0 != 0)) * 2, ff1(__ballot(mis1 != 0)) * 2 + 1) | (w & 3) * 128);
    asm volatile("s_waitcnt lgkmcnt(0)\n\ts_barrier" ::: "memory");
#pragma unroll
    for (int x = 0; x < 2; ++x) { const int row = 8 * (w & 3) + 4 * x + g; lds_st<u32x4>(lds, L_Q + row * 256 + ((c ^ (row & 15)) * 16), qv[x]); }
    asm volatile("s_waitcnt lgkmcnt(0)\n\ts_barrier" : "+v"(cxv) :: "memory");
    asm volatile("" :: "v"(pf));
  }
  cxv = min(max(cxv, 0), LK);
  const int npv = (cxv + PAGE - 1) / PAGE;
  int npmax;
  {
    int t = max(npv, row_shr<1>(npv)); t = max(t, row_shr<2>(t)); t = max(t, row_shr<4>(t));
    npmax = __builtin_amdgcn_readlane(t, 7);
  }
  int npm = npv - team(lane & 7) * NSTR;
  asm volatile("" : "+v"(npm), "+s"(npmax));
  if (w3 < NSTR) asm volatile("s_barrier" ::: "memory");
  int S;
  {
    const u32x4 f0 = lds_ld<u32x4>(lds, L_FM);
    S = (int)min((unsigned)__builtin_amdgcn_readfirstlane((int)min(min(f0[0], f0[1]), min(f0[2], f0[3]))), (unsigned)npmax);
  }
  const int call = max(npm - S, 0);
  int Wt = call + row_shr<1>(call); Wt += row_shr<2>(Wt); Wt += row_shr<4>(Wt);
  Wt = __builtin_amdgcn_readlane(Wt, 7) + S;
  const int Sa = (S * SHR) >> 8;
  const int cnt = max(call - (((Wt + Sa + (nw > 32 ? EXS : EXTRA) * (nw - B)) * rnw) >> 16), 0);
  int se = cnt + row_shr<1>(cnt); se += row_shr<2>(se); se += row_shr<4>(se); se += S;
  const int sb = se - cnt;
  const int Wp = __builtin_amdgcn_readlane(se, 7), wr = (Wp + Sa) * rnc;
  auto cut = [&](int j) { const int y = (j - B) * wr; return min(max(((y >> 8) * (65536 / (256 + SHR))) >> 16, (y >> 16) - Sa), Wp); };
  const bool own = wi < B;
  const int c0 = own ? 0 : cut(wi);
  const int c1 = own ? __builtin_amdgcn_readlane(call - cnt, bh) : cut(wi + 1);
  const int priv = own ? 0 : (int)(__ballot(max(sb, c0) < min(se, c1)) & 0xFF);
  const int wgmask = (!own && c0 < S && c0 < c1) ? 0xFF : priv | 1 << bh;
  const int n = (c1 - c0 - w + 2 * NSTR - 1) >> 2;
  if (__builtin_expect(w >= NSTR, 0)) {
    const int r = w - NSTR, lb = (int)(uintptr_t)lds, hf = lane >> 5, vo = r * 512 + (lane & 31) * 16;
    const int hb = WS_PART + hs * AREA, mine = hb + wi * AREA;
    tag += TAG_STEP;
    if (!own) {
      int bp[4], a_x[4], vs[4]; unsigned tq[4];
      {
        int pm = wgmask;
#pragma unroll
        for (int u = 0; u < 4; ++u) {
          const int p1 = pm & (pm - 1), pl = hf ? p1 : pm, b = __builtin_ctz(pl | 0x80);
          bp[u] = b * 16; a_x[u] = lb + L_RING + b * SEQ + r * ROW + (lane & 31) * 16; vs[u] = pl ? mine + b * 2048 + vo : WS_BYTES;
          tq[u] = __builtin_amdgcn_ds_bpermute(bp[u], tag);
          pm = p1 & (p1 - 1);
          asm volatile("" : "+v"(bp[u]), "+v"(a_x[u]), "+v"(vs[u]), "+v"(tq[u]));
        }
      }
      int a_ml = lb + L_ML + (((lane >> 2) & 7) * 4 + r) * 32 + (lane & 3) * 8;
      int vm = (lane & 35) == 0 && ((wgmask >> (lane >> 2)) & 1) ? mine + A_ML + (lane + r) * 16 : WS_BYTES;
      const bool many = __builtin_popcount(wgmask) > 2;
      {
        int a_cnt = lb + L_CNT;
        asm volatile("" : "+v"(a_ml), "+v"(vm), "+v"(a_cnt));
        while (__builtin_amdgcn_readfirstlane(lds_vld<int>(lds, L_CNT + 8)) != ALL_DONE) __builtin_amdgcn_s_sleep(4);
        u32x2 fl; f32x2 ml; f32x4 xv[NSTR];
#pragma unroll 1
        for (;;) {
          rec_reads(fl, ml, xv, a_cnt, a_ml, a_x[0]);
          if (__builtin_amdgcn_readfirstlane(fl[0]) == ALL_DONE) break;
          __builtin_amdgcn_s_sleep(1);
        }
        const float e = quad_max(ml[0]);
        const float wt = __builtin_amdgcn_exp2f(ml[0] - e);
        float wk[4][NSTR];
#pragma unroll
        for (int u = 0; u < 4; ++u)
#pragma unroll
          for (int k = 0; k < NSTR; ++k) wk[u][k] = bperm(bp[u] + 4 * k, wt);
        const unsigned ls = __float_as_uint(quad_sum(wt * ml[1])), en = __float_as_uint(e) ^ NEG;
        __builtin_amdgcn_raw_buffer_store_b128(u32x4{en, ls, en ^ tag, ls ^ tag}, rw, vm, 0, 16);
#pragma unroll
        for (int u = 0; u < 4; ++u) asm volatile("" : "+v"(wk[u][0]), "+v"(wk[u][1]), "+v"(wk[u][2]), "+v"(wk[u][3]));
        rec_wait(fl[1], xv, a_cnt, a_x[0]);
        f32x4 xa[3][NSTR];
        if (many) {
#pragma unroll
          for (int u = 1; u < 4; ++u)
#pragma unroll
            for (int k = 0; k < NSTR; ++k) xa[u - 1][k] = lds_vld<f32x4>((ldsp)0, a_x[u] + k * RECS);
        }
        __builtin_amdgcn_raw_buffer_store_b128(seal(wk[0][0] * xv[0] + wk[0][1] * xv[1] + (wk[0][2] * xv[2] + wk[0][3] * xv[3]), tq[0]), rw, vs[0], 0, 16);
        if (many) {
#pragma unroll
          for (int u = 1; u < 4; ++u)
            __builtin_amdgcn_raw_buffer_store_b128(seal(wk[u][0] * xa[u - 1][0] + wk[u][1] * xa[u - 1][1] + (wk[u][2] * xa[u - 1][2] + wk[u][3] * xa[u - 1][3]), tq[u]), rw, vs[u], 0, 16);
        }
      }
    } else {
      const int b = bh, row = b * 4 + r, dm = (lane & 31) * 4 + hf * 2;
      const unsigned tg = __builtin_amdgcn_readlane(tag, 4 * b);
      const int cj = cut(lane), cn = cut(lane + 1);
      const bool con = lane < nw && lane >= B && ((cj < S && cj < cn) || ((lane + ROT * (lane >> 3)) & 7) == b || max(cj, __builtin_amdgcn_readlane(sb, b)) < min(cn, __builtin_amdgcn_readlane(se, b)));
      const unsigned long long late = __ballot(con);
      const int cx = __builtin_amdgcn_readlane(cxv, b);
      int a_ml = lb + L_ML + row * 32 + (lane & 3) * 8, a_x = lb + L_RING + b * SEQ + r * ROW + dm * 4, spin = 0;
      {
        int a_cnt = lb + L_CNT, vm = con ? lane * AREA + A_ML + row * 16 : WS_BYTES;
        unsigned tm = con ? tg : 0;
        unsigned short* op = out + (b * H + kvh * G + r) * D + dm;
        unsigned long long pb = late;
        u32x2 fl; f32x2 ml, xv[NSTR];
        float E, Lx, M, Lt, ae, wt;
        f32x4 sx = {0.f, 0.f, 0.f, 0.f};
        f32x2 ow; float inv; int slow;
        constexpr float MASKED = -998244352.0f * 1.4426950408889634f;
        auto own_sum = [&](float w0, float w1, float w2, float w3) {
#pragma unroll
          for (int i = 0; i < 2; ++i) ow[i] = __builtin_fmaf(w0, xv[0][i], w1 * xv[1][i]) + __builtin_fmaf(w2, xv[2][i], w3 * xv[3][i]);
        };
        auto scale = [&]() {
          M = fmaxf(E, quad_max(ml[0]));
          ae = __builtin_amdgcn_exp2f(E - M);
          wt = __builtin_amdgcn_exp2f(ml[0] - M);
          Lt = __builtin_fmaf(ae, Lx, quad_sum(wt * ml[1]));
          asm volatile("" : "+v"(Lt), "+v"(ae), "+v"(wt), "+v"(M));
        };
#pragma unroll 1
        do {
          int so[8], jl[8], mk[8];
#pragma unroll
          for (int u = 0; u < 8; ++u) {
            const unsigned long long p1 = pb & (pb - 1);
            const int j1 = ff1(p1), j0 = ff1(pb), j = hf ? j1 : j0;
            so[u] = j < 0 ? WS_BYTES : j * AREA + b * 2048 + vo; jl[u] = max(j, 0) * 4; mk[u] = j < 0 ? 0 : 255;
            pb = p1 & (p1 - 1);
            asm volatile("" : "+v"(so[u]), "+v"(jl[u]), "+v"(mk[u]));
          }
          asm volatile("" : "+v"(a_ml), "+v"(a_x), "+v"(op), "+v"(a_cnt), "+v"(vm), "+v"(tm));
          while (__builtin_amdgcn_readfirstlane(lds_vld<int>(lds, L_CNT + 8)) != ALL_DONE) __builtin_amdgcn_s_sleep(XD);
          float wq[8];
          f32x4 sn;
          int todo = 1;
#pragma unroll 1
          for (;;) {
            asm volatile("" ::: "memory");
            const u32x4 gm = __builtin_amdgcn_raw_buffer_load_b128(rw, vm, hb, 16);
            u32x4 gx[8];
#pragma unroll
            for (int u = 0; u < 8; ++u) gx[u] = __builtin_amdgcn_raw_buffer_load_b128(rw, so[u], hb, 16);
            asm volatile("" :: "v"(gm));
            if (todo) {
              todo = __ballot(((gm[0] ^ gm[2] ^ tm) | (gm[1] ^ gm[3] ^ tm)) != 0) != 0;
              const float e = __uint_as_float(gm[0] ^ NEG);
              E = xg_max(row_max(e));
              const float wj = vm == WS_BYTES ? 0.f : __builtin_amdgcn_exp2f(e - E);
#pragma unroll
              for (int u = 0; u < 8; ++u) wq[u] = bperm(jl[u], wj);
              Lx = xg_sum(row_sum(wj * __uint_as_float(gm[1])));
            }
            rec_reads(fl, ml, xv, a_cnt, a_ml, a_x);
            scale();
            own_sum(rdl(wt, 0), rdl(wt, 1), rdl(wt, 2), rdl(wt, 3));
            inv = __builtin_amdgcn_rcpf(Lt);
            slow = __builtin_amdgcn_readfirstlane((int)((fl[0] & fl[1]) != ALL_DONE || MASKED - M > -200.f));
            asm volatile("" : "+v"(ow), "+v"(inv), "+s"(slow));
            unsigned bad = 0;
            sn = sx;
#pragma unroll
            for (int u = 0; u < 8; ++u) {
              u32x4 x;
#pragma unroll
              for (int i = 0; i < 4; ++i) x[i] = gx[u][i] ^ ((tg >> (8 * i)) & 255);
              bad |= __builtin_amdgcn_bitop3_b32(x[0] | x[1] | x[2], x[3], mk[u], 0xA8);
              sn += wq[u] * __builtin_bit_cast(f32x4, x);
            }
            asm volatile("" : "+v"(sn));
            if ((!todo && !__ballot(bad != 0)) || ++spin > (1 << 20)) break;
          }
          sx = sn;
        } while (pb);
        f32x2 o;
#pragma unroll
        for (int i = 0; i < 2; ++i) {
          auto t = __builtin_amdgcn_permlane32_swap(__float_as_uint(sx[i]), __float_as_uint(sx[i + 2]), false, false);
          o[i] = __uint_as_float(t[0]) + __uint_as_float(t[1]);
        }
        if (slow) {
#pragma unroll 1
          while (__builtin_amdgcn_readfirstlane(fl[0]) != ALL_DONE) { __builtin_amdgcn_s_sleep(1); rec_reads(fl, ml, xv, a_cnt, a_ml, a_x); scale(); }
          if (__builtin_expect(cx < LK && MASKED - M > -200.f, 0)) {
            f32x2 om = {0.f, 0.f};
            for (int p = cx; p < LK; ++p) {
              int ph = gld<int>(bt + (b * NB + (p >> 4)) * 4);
              ph = ph < 0 ? ph + nphys : ph;
              const unsigned vv = gld<unsigned>(vc + (size_t)ph * PAGE_BYTES + (p & 15) * 2048 + kvh * 256 + dm * 2);
              om += f32x2{__uint_as_float(vv << 16), __uint_as_float(vv & 0xFFFF0000u)};
            }
            const float Mm = fmaxf(M, MASKED), wm = __builtin_amdgcn_exp2f(MASKED - Mm), a = __builtin_amdgcn_exp2f(M - Mm);
            Lt = Lt * a + wm * (float)(LK - cx);
            o = o * (a * ae) + wm * om; ae = 1.f;
            wt *= a;
          }
          inv = __builtin_amdgcn_rcpf(Lt);
          float w0 = rdl(wt, 0), w1 = rdl(wt, 1), w2 = rdl(wt, 2), w3 = rdl(wt, 3);
          asm volatile("" : "+s"(w0), "+s"(w1), "+s"(w2), "+s"(w3), "+v"(o), "+v"(inv));
          rec_wait(fl[1], xv, a_cnt, a_x);
          own_sum(w0, w1, w2, w3);
        }
#pragma unroll
        for (int i = 0; i < 2; ++i) o[i] = __builtin_fmaf(o[i], ae, ow[i]) * inv;
        const unsigned res = cvtpk(o[0], o[1]);
        asm volatile("global_store_dword %0, %1, off sc0 sc1" :: "v"(op), "v"(res) : "memory");
      }
      if (lane == 0) __hip_atomic_store(epoch + b, tg, __ATOMIC_RELAXED, __HIP_MEMORY_SCOPE_AGENT);
    }
    __builtin_amdgcn_s_waitcnt(0);
    __builtin_amdgcn_endpgm();
  }
  {
    unsigned dphys; int djb;
    const int fw = w + NSTR * lane - NSTR;
    {
      const int f = (wi & DESC) ? max(c1 - 1 - fw, 0) : min(c0 + fw, c1 - 1);
      const int dl = S + (lane & 7) * NB - sb;
      int idx = f + (own ? __builtin_amdgcn_readlane(dl + se, bh) : 0);
      for (int pm = priv; pm; pm &= pm - 1) {
        const int b = __builtin_ctz(pm);
        const int s0 = __builtin_amdgcn_readlane(sb, b), d0 = __builtin_amdgcn_readlane(dl, b);
        idx = f >= s0 ? f + d0 : idx;
      }
      int ph = lds_ld<int>(lds, L_TBL + (idx & (B * NB - 1)) * 4);
      ph = ph < 0 ? ph + nphys : ph;
      dphys = (unsigned)ph * PAGE_BYTES;
      djb = ((idx & (NB - 1)) * PAGE) | (idx < S ? 0xFF0000 : 0x10000 << ((idx >> 9) & 7));
    }
    asm volatile("" : "+v"(dphys));
    if (n > 1) {
      const unsigned so = __builtin_amdgcn_readlane(dphys, 1);
      dma4(kvo, rk, so, ring + SLOT);
      dma4(vvo, rv, so, ring + SLOT + VOFF);
    }
    const int jh = j0 >= S * PAGE && j0 < __builtin_amdgcn_readlane(cxv, bh) ? j0 | (0x10000 << bh) : 0;
    djb = lane ? djb : jh;
#pragma unroll
    for (int x = 0; x < 2; ++x) limv[x] = min(max(limv[x], 0), LK) - 4 * g;
    asm volatile("s_barrier" ::: "memory");
    u32x4 qf[2][4];
#pragma unroll
    for (int x = 0; x < 2; ++x)
#pragma unroll
      for (int kk = 0; kk < 4; ++kk) qf[x][kk] = lds_ld<u32x4>(lds, L_Q + (x * 16 + c) * 256 + (((4 * kk + g) ^ c) * 16));
    int bbit[2];
#pragma unroll
    for (int x = 0; x < 2; ++x) bbit[x] = 1 << (16 + 4 * x + (c >> 2));
    int ka[4], va[8];
#pragma unroll
    for (int kk = 0; kk < 4; ++kk) ka[kk] = w * RECS + c * 256 + (((4 * kk + g) ^ c) * 16);
#pragma unroll
    for (int d = 0; d < 8; ++d) va[d] = w * RECS + (4 * g + (c >> 2)) * 256 + ((((4 * g + (c >> 2)) & 7) ^ d) << 5) + ((c & 3) << 3);
    int a_rec[2], a_mlw = L_ML + c * 32 + w * 8, a_fl = L_CNT + w, one = 1;
#pragma unroll
    for (int x = 0; x < 2; ++x) a_rec[x] = L_RING + w * RECS + (4 * x + (c >> 2)) * SEQ + (c & 3) * ROW + g * 16;
    asm volatile("" : "+v"(a_rec[0]), "+v"(a_rec[1]), "+v"(a_mlw), "+v"(a_fl), "+v"(one));

    f32x4 acc[2][8];
    float m[2], l[2];
#pragma unroll
    for (int x = 0; x < 2; ++x) { m[x] = -FLT_MAX; l[x] = 0.f;
#pragma unroll
      for (int d = 0; d < 8; ++d) acc[x][d] = f32x4{0.f, 0.f, 0.f, 0.f}; }

    asm volatile("" : "+v"(qf[0][0]), "+v"(qf[0][1]), "+v"(qf[0][2]), "+v"(qf[0][3]), "+v"(qf[1][0]), "+v"(qf[1][1]), "+v"(qf[1][2]), "+v"(qf[1][3]));
    const int near = own ? NEARO : NEAR;
#pragma unroll 1
    for (int i = 0; i < n; ++i) {
      {
        const int step = (i & (NS - 1)) == NS - 1 ? (1 - NS) * SLOT : SLOT;
        const int jb = __builtin_amdgcn_readlane(djb, i), rem = n - i;
        if (rem > 2) {
          const unsigned so = __builtin_amdgcn_readlane(dphys, i + 2), sa = ring + ((i + 2) & (NS - 1)) * SLOT;
          dma4(kvo, rk, so, sa);
          dma4(vvo, rv, so, sa + VOFF);
        } else {
          asm volatile("s_waitcnt vmcnt(12)" ::: "memory");
          if (rem < 2) asm volatile("s_waitcnt vmcnt(4)" ::: "memory");
        }
        asm volatile("s_waitcnt vmcnt(20)" ::: "memory");
        u32x4 kf[4];
#pragma unroll
        for (int kk = 0; kk < 4; ++kk) kf[kk] = lds_ld<u32x4>(lds, ka[kk] + L_RING);
#pragma unroll
        for (int kk = 0; kk < 4; ++kk) ka[kk] += step;
        u32x2 pb[2];
#pragma unroll
        for (int x = 0; x < 2; ++x) {
          if (jb & (COMPM << (4 * x))) {
            f32x4 s = {0.f, 0.f, 0.f, 0.f};
#pragma unroll
            for (int kk = 0; kk < 4; ++kk)
              s = __builtin_amdgcn_mfma_f32_16x16x32_bf16(__builtin_bit_cast(bf16x8, kf[kk]), __builtin_bit_cast(bf16x8, qf[x][kk]), s, 0, 0, 0);
            const int lim = (jb & bbit[x]) ? limv[x] - (jb & 0xFFFF) : 0;
#pragma unroll
            for (int r = 0; r < 4; ++r) s[r] = (r < lim) ? s[r] : -INFINITY;
            float mx = fmaxf(fmaxf(s[0], s[1]), fmaxf(s[2], s[3]));
            mx = xg_max(mx) * CSC;
            const float mn = fmaxf(m[x], mx);
            const float al = __builtin_amdgcn_exp2f(m[x] - mn);
            m[x] = mn;
            float p[4];
#pragma unroll
            for (int r = 0; r < 4; ++r) p[r] = __builtin_amdgcn_exp2f(__builtin_fmaf(s[r], CSC, -mn));
            l[x] = __builtin_fmaf(l[x], al, (p[0] + p[1]) + (p[2] + p[3]));
            pb[x] = u32x2{cvtpk(p[0], p[1]), cvtpk(p[2], p[3])};
#pragma unroll
            for (int d = 0; d < 8; ++d) acc[x][d] *= al;
          }
        }
        if (rem <= near) {
          lds_vst<unsigned char>(lds, a_fl + 8, (unsigned char)one);
          if (rem == 1) {
#pragma unroll
            for (int x = 0; x < 2; ++x) lds_vst<f32x2>(lds, a_mlw + x * 512, f32x2{m[x], xg_sum(l[x])});
            lds_vst<unsigned char>(lds, a_fl, (unsigned char)one);
            asm volatile("s_waitcnt vmcnt(0)" ::: "memory");
          }
        }
        if (rem < 3) asm volatile("s_waitcnt vmcnt(8)" ::: "memory");
        asm volatile("s_waitcnt vmcnt(16)" ::: "memory");
        s16x4 vt[8];
#pragma unroll
        for (int d = 0; d < 8; ++d) {
          typedef __attribute__((ext_vector_type(4))) short v4i16;
          vt[d] = __builtin_amdgcn_ds_read_tr16_b64_v4i16((__attribute__((address_space(3))) v4i16*)(lds + (va[d] + L_RING + VOFF)));
        }
        asm volatile("s_waitcnt lgkmcnt(0)" : "+v"(vt[0]), "+v"(vt[1]), "+v"(vt[2]), "+v"(vt[3]), "+v"(vt[4]), "+v"(vt[5]), "+v"(vt[6]), "+v"(vt[7]) :: "memory");
#pragma unroll
        for (int d = 0; d < 8; ++d) va[d] += step;
#pragma unroll
        for (int x = 0; x < 2; ++x) {
          if (jb & (COMPM << (4 * x))) {
#pragma unroll
            for (int d = 0; d < 8; ++d)
              acc[x][d] = __builtin_amdgcn_mfma_f32_16x16x16bf16_1k(vt[d], __builtin_bit_cast(s16x4, pb[x]), acc[x][d], 0, 0, 0);
          }
        }
        if (rem == 1) {
#pragma unroll
          for (int x = 0; x < 2; ++x) {
            if (wgmask & (0xF << (4 * x))) {
#pragma unroll
              for (int d = 0; d < 8; ++d) lds_vst<f32x4>(lds, a_rec[x] + d * 64, acc[x][d]);
            }
          }
          lds_vst<unsigned char>(lds, a_fl + 4, (unsigned char)one);
        }
      }
    }
  }
}
}

extern "C" void kernel_launch(void* const* d_in, const int* in_sizes, int n_in, void* d_out, int out_size, void* d_ws, size_t ws_size,
                              hipStream_t stream) {
  const unsigned bytes = (unsigned)in_sizes[1] * 2, fmt = 0x00020000u;
  attn_decode<<<256, NTHR, 0, stream>>>(d_in[1], bytes, fmt, d_in[2], bytes, fmt, d_in[0], d_in[3], d_in[4], d_ws, (unsigned short*)d_out);
}
```
